```python
import torch

def _softmax(x, axis=-1):
    return torch.softmax(x, dim=axis)
def _gelu(x):
    return torch.nn.functional.gelu(x, approximate="tanh")
def _silu(x):
    return torch.nn.functional.silu(x)
def _sigmoid(x):
    return torch.sigmoid(x)
def _layernorm(x, axis=-1, eps=1e-5):
    m = x.mean(dim=axis, keepdim=True)
    v = x.var(dim=axis, keepdim=True, unbiased=False)
    return (x - m) / torch.sqrt(v + eps)
def _scatter_add(tgt, idx, src, axis=0):
    return tgt.clone().index_add_(axis, idx, src)
def _segment_sum(src, seg, n):
    out = torch.zeros((n,) + src.shape[1:], dtype=src.dtype)
    return out.index_add_(0, seg, src)
def _pad(x, pw):
    flat = tuple(v for pair in reversed(pw) for v in pair)
    return torch.nn.functional.pad(x, flat)

kernel_name = 'recurrent_scan__ssm_conv_s5__dense_activation_silu__prompt'

def setup_inputs():

    _w = (lambda a: (lambda t: t.to(torch.bfloat16) if t.is_floating_point() else t)(torch.as_tensor(a)))
    def _A_setup():
        B, S, D = 1, 2048, 640
        P = D
        Lambda_re = -0.5 * (lambda _x: torch.exp(torch.as_tensor(_x)))((lambda *s: torch.randn(*s, dtype=torch.bfloat16) * 0.8)(P))
        Lambda_im = (lambda *s: torch.randn(*s, dtype=torch.bfloat16) * 0.8)(P) * 1.0
        B_re = (lambda *s: torch.randn(*s, dtype=torch.bfloat16) * 0.8)(P, D) * (D ** -0.5)
        B_im = (lambda *s: torch.randn(*s, dtype=torch.bfloat16) * 0.8)(P, D) * (D ** -0.5)
        C_re = (lambda *s: torch.randn(*s, dtype=torch.bfloat16) * 0.8)(D, P) * (P ** -0.5)
        C_im = (lambda *s: torch.randn(*s, dtype=torch.bfloat16) * 0.8)(D, P) * (P ** -0.5)
        log_dt = (lambda *s: torch.randn(*s, dtype=torch.bfloat16) * 0.8)(P) * 0.3 - 2.0
        Dskip = (lambda *s: torch.randn(*s, dtype=torch.bfloat16) * 0.4)(D)
        return {
            "x": _w((lambda *s: torch.randn(*s, dtype=torch.bfloat16) * 0.05)(B, S, D)),
            "Lambda_re": _w(Lambda_re),
            "Lambda_im": _w(Lambda_im),
            "B_re": _w(B_re),
            "B_im": _w(B_im),
            "C_re": _w(C_re),
            "C_im": _w(C_im),
            "log_dt": _w(log_dt),
            "Dskip": _w(Dskip),
        }
    _A_in = _A_setup()

    def _B_setup():
        B, S, D, F = 1, 2048, 640, 1536
        return {
            "x": _w((lambda *s: torch.randn(*s, dtype=torch.bfloat16) * 0.05)(B, S, D)),
            "w1": _w((lambda *s: torch.randn(*s, dtype=torch.bfloat16) * 0.8)(D, F) * (D ** -0.5)),
            "w2": _w((lambda *s: torch.randn(*s, dtype=torch.bfloat16) * 0.1)(F, D) * (F ** -0.5)),
        }
    _B_in = _B_setup()

    return {
        "A_x": _A_in["x"],
        "A_Lambda_re": _A_in["Lambda_re"],
        "A_Lambda_im": _A_in["Lambda_im"],
        "A_B_re": _A_in["B_re"],
        "A_B_im": _A_in["B_im"],
        "A_C_re": _A_in["C_re"],
        "A_C_im": _A_in["C_im"],
        "A_log_dt": _A_in["log_dt"],
        "A_Dskip": _A_in["Dskip"],
        "B_x": _B_in["x"],
        "B_w1": _B_in["w1"],
        "B_w2": _B_in["w2"],
    }

def reference(A_x, A_Lambda_re, A_Lambda_im, A_B_re, A_B_im, A_C_re, A_C_im, A_log_dt, A_Dskip, B_x, B_w1, B_w2):
    def _A_ref(x, Lambda_re, Lambda_im, B_re, B_im, C_re, C_im, log_dt, Dskip):
        Bsz, L, H = x.shape
        Lam = Lambda_re + 1j * Lambda_im
        Bmat = B_re + 1j * B_im
        Cmat = C_re + 1j * C_im
        dt = torch.exp(log_dt)
        Lam_bar = torch.exp(dt * Lam)
        B_bar = ((Lam_bar - 1.0) / Lam)[:, None] * Bmat
        Bu = torch.einsum("bld,pd->blp", x + 0j, B_bar)
        h = torch.zeros_like(Bu[:, 0, :])
        hs = []
        for t in range(L):
            h = Lam_bar[None, :] * h + Bu[:, t, :]
            hs.append(h)
        hseq = torch.stack(hs, 1)
        y = 2.0 * torch.einsum("blp,dp->bld", hseq, Cmat).real + x * Dskip[None, None, :]
        return y

    def _B_ref(x, w1, w2):
        h = torch.einsum("bsd,df->bsf", x, w1)
        h = _silu(h)
        out = torch.einsum("bsf,fd->bsd", h, w2)
        return out

    x = A_x
    xn = _layernorm(x, axis=-1)
    s = _A_ref(xn, A_Lambda_re, A_Lambda_im, A_B_re, A_B_im, A_C_re, A_C_im, A_log_dt, A_Dskip)
    g = _silu(_B_ref(xn, B_w1, B_w2))
    return x + s * g
```

```cpp
#include <hip/hip_runtime.h>
#include <hip/hip_bf16.h>
#include <stdint.h>

typedef __attribute__((ext_vector_type(8))) short bf16x8;
typedef __attribute__((ext_vector_type(4))) float f32x4;
typedef __attribute__((ext_vector_type(2))) float f32x2;
typedef __attribute__((ext_vector_type(4))) unsigned u32x4;
typedef __attribute__((ext_vector_type(2))) unsigned u32x2;
typedef __attribute__((ext_vector_type(2))) __bf16 bf2_t;
#define GAS __attribute__((address_space(1)))
typedef GAS unsigned long long gu64;

#define WAIT_V(n) asm volatile("s_waitcnt vmcnt(%0)" ::"n"(n) : "memory")
#define WAIT_L(n) asm volatile("s_waitcnt lgkmcnt(%0)" ::"n"(n) : "memory")
#define SCHED() __builtin_amdgcn_sched_barrier(0)

constexpr int L_ = 2048, D_ = 640, P_ = 640, F_ = 1536;
constexpr size_t OFF_XNF = 0;
constexpr size_t OFF_WBF = OFF_XNF + 2621440;
constexpr size_t OFF_W1F = OFF_WBF + 1638400;
constexpr size_t OFF_CF  = OFF_W1F + 1966080;
constexpr size_t OFF_W2F = OFF_CF + 1638400;
constexpr size_t OFF_HF  = OFF_W2F + 1966080;
constexpr size_t OFF_A1F = OFF_HF + 5242880;
constexpr size_t OFF_TAB = OFF_A1F + 6291456;
constexpr size_t OFF_GRN = OFF_TAB + 102400;
constexpr int T_COEF = 0, T_L1 = 1, T_L64 = 2, T_L128 = 3, T_P4 = 4;
constexpr unsigned long long SENT = 0xFFFFFFFFFFFFFFFFull;

__device__ __forceinline__ float bf2f(unsigned short v) { return __uint_as_float((unsigned)v << 16); }
__device__ __forceinline__ float bfr(float a) { return (float)(__bf16)a; }
__device__ __forceinline__ unsigned pack2(float a, float b) {
  bf2_t v = {(__bf16)a, (__bf16)b};
  return __builtin_bit_cast(unsigned, v);
}
__device__ __forceinline__ float lo_f(unsigned u) { return __uint_as_float(u << 16); }
__device__ __forceinline__ float hi_f(unsigned u) { return __uint_as_float(u & 0xFFFF0000u); }
__device__ __forceinline__ float silu_f(float x) { return x * __builtin_amdgcn_rcpf(1.f + __expf(-x)); }
__device__ __forceinline__ f32x2 cmul(f32x2 a, f32x2 b) {
  f32x2 r; r.x = a.x * b.x - a.y * b.y; r.y = a.x * b.y + a.y * b.x; return r;
}
__device__ __forceinline__ f32x2 cfma(f32x2 a, f32x2 b, f32x2 c) {
  f32x2 r; r.x = fmaf(a.x, b.x, fmaf(-a.y, b.y, c.x)); r.y = fmaf(a.x, b.y, fmaf(a.y, b.x, c.y)); return r;
}
template <int N> __device__ __forceinline__ float row_shr(float v) {
  return __builtin_bit_cast(float, __builtin_amdgcn_update_dpp(0, __builtin_bit_cast(int, v), 0x110 + N, 0xf, 0xf, true));
}
template <int N> __device__ __forceinline__ f32x2 row_shr2(f32x2 v) { f32x2 r; r.x = row_shr<N>(v.x); r.y = row_shr<N>(v.y); return r; }

constexpr int PREP_LN = 128, PREP_PACK = 440;
__global__ __launch_bounds__(256) void prep_kernel(
    const unsigned short* __restrict__ x, const unsigned short* __restrict__ lre, const unsigned short* __restrict__ lim,
    const unsigned short* __restrict__ Bre, const unsigned short* __restrict__ Bim,
    const unsigned short* __restrict__ Cre, const unsigned short* __restrict__ Cim,
    const unsigned short* __restrict__ logdt, const unsigned short* __restrict__ w1, const unsigned short* __restrict__ w2,
    char* __restrict__ ws) {
  __shared__ float red[2][4][16];
  const int tid = threadIdx.x, wid = tid >> 6, lane = tid & 63, g = lane >> 4, i = lane & 15;
  const int blk = blockIdx.x;
  if (blk < PREP_LN) {
    const int b = blk >> 2, mt = blk & 3, t = 64 * b + 4 * i + mt;
    u32x4 v[5];
#pragma unroll
    for (int q = 0; q < 5; ++q) v[q] = *(const u32x4*)(x + (size_t)t * D_ + 32 * (wid + 4 * q) + 8 * g);
    float s = 0.f;
#pragma unroll
    for (int q = 0; q < 5; ++q)
#pragma unroll
      for (int e = 0; e < 4; ++e) s += lo_f(v[q][e]) + hi_f(v[q][e]);
    s += __shfl_xor(s, 16); s += __shfl_xor(s, 32);
    if (lane < 16) red[0][wid][lane] = s;
    __syncthreads();
    const float mean = (red[0][0][i] + red[0][1][i] + red[0][2][i] + red[0][3][i]) * (1.f / D_);
    float ss = 0.f;
#pragma unroll
    for (int q = 0; q < 5; ++q)
#pragma unroll
      for (int e = 0; e < 4; ++e) { float a = lo_f(v[q][e]) - mean, c = hi_f(v[q][e]) - mean; ss += a * a + c * c; }
    ss += __shfl_xor(ss, 16); ss += __shfl_xor(ss, 32);
    if (lane < 16) red[1][wid][lane] = ss;
    __syncthreads();
    const float var = (red[1][0][i] + red[1][1][i] + red[1][2][i] + red[1][3][i]) * (1.f / D_);
    const float mb = bfr(mean);
    const float den = bfr(sqrtf(bfr(bfr(var) + 1e-5f)));
    char* dst = ws + OFF_XNF + (size_t)(b >> 1) * 163840 + (size_t)((b & 1) * 8 + mt * 2) * 1024 + lane * 16;
#pragma unroll
    for (int q = 0; q < 5; ++q) {
      const int kb = wid + 4 * q;
      u32x4 o;
#pragma unroll
      for (int e = 0; e < 4; ++e) {
        float a = bfr(lo_f(v[q][e]) - mb) / den, c = bfr(hi_f(v[q][e]) - mb) / den;
        o[e] = pack2(a, c);
      }
      *(u32x4*)(dst + (size_t)(kb >> 1) * 16384 + (kb & 1) * 1024) = o;
    }
  } else if (blk < PREP_LN + PREP_PACK) {
#pragma unroll 1
    for (int q = 0; q < 4; ++q) {
      int f = (blk - PREP_LN) * 16 + wid * 4 + q;
      u32x4 o;
      char* dst;
      if (f < 1600) {
        int kb2 = f & 1, nt = (f >> 1) % 10, ks = (f / 20) % 10, ct = f / 200;
        int ch = 80 * ct + 8 * nt + (i >> 1);
        const unsigned short* src = ((i & 1) ? Bim : Bre) + (size_t)ch * D_ + 64 * ks + 32 * kb2 + 8 * g;
        o = *(const u32x4*)src;
        dst = ws + OFF_WBF + (size_t)f * 1024;
      } else if (f < 3520) {
        f -= 1600;
        int kb2 = f & 1, nt = (f >> 1) % 12, ks = (f / 24) % 10, ft = f / 240;
        int col = 192 * ft + 16 * nt + i, k0 = 64 * ks + 32 * kb2 + 8 * g;
        const unsigned short* src = w1 + (size_t)k0 * F_ + col;
#pragma unroll
        for (int e = 0; e < 4; ++e) o[e] = (unsigned)src[(2 * e) * F_] | ((unsigned)src[(2 * e + 1) * F_] << 16);
        dst = ws + OFF_W1F + (size_t)f * 1024;
      } else if (f < 5120) {
        f -= 3520;
        int nt = f % 5, KB = (f / 5) % 40, bn = f / 200;
        int d = 80 * bn + 16 * nt + i;
#pragma unroll
        for (int h = 0; h < 2; ++h) {
          int c = 16 * KB + 8 * h + 2 * g;
          unsigned re = *(const unsigned*)(Cre + (size_t)d * P_ + c);
          unsigned im = *(const unsigned*)(Cim + (size_t)d * P_ + c) ^ 0x80008000u;
          o[2 * h] = (re & 0xFFFFu) | (im << 16);
          o[2 * h + 1] = (re >> 16) | (im & 0xFFFF0000u);
        }
        dst = ws + OFF_CF + (size_t)f * 1024;
      } else {
        f -= 5120;
        int nt = f % 5, KB = (f / 5) % 48, bn = f / 240;
        int d = 80 * bn + 16 * nt + i;
#pragma unroll
        for (int h = 0; h < 2; ++h) {
          const unsigned short* src = w2 + (size_t)(32 * KB + 16 * h + 4 * g) * D_ + d;
          o[2 * h] = (unsigned)src[0] | ((unsigned)src[D_] << 16);
          o[2 * h + 1] = (unsigned)src[2 * D_] | ((unsigned)src[3 * D_] << 16);
        }
        dst = ws + OFF_W2F + (size_t)f * 1024;
      }
      *(u32x4*)(dst + lane * 16) = o;
    }
  } else {
    f32x2* tab = (f32x2*)(ws + OFF_TAB);
    for (int p = tid; p < P_; p += 256) {
      const float dt = bfr(expf(bf2f(logdt[p])));
      const float ar = bf2f(lre[p]), ai = bf2f(lim[p]);
      const float zr = dt * ar, zi = dt * ai;
      const float ex = expf(zr);
      f32x2 L; L.x = ex * cosf(zi); L.y = ex * sinf(zi);
      const float nr = L.x - 1.f, ni = L.y, dd = ar * ar + ai * ai;
      f32x2 cf; cf.x = (nr * ar + ni * ai) / dd; cf.y = (ni * ar - nr * ai) / dd;
      tab[T_COEF * P_ + p] = cf;
      tab[T_L1 * P_ + p] = L;
      f32x2 L2 = cmul(L, L), L4 = cmul(L2, L2);
      f32x2 pw; pw.x = 1.f; pw.y = 0.f;
      for (int k = 0; k < 16; ++k) { tab[(T_P4 + k) * P_ + p] = pw; pw = cmul(pw, L4); }
      f32x2 L8 = cmul(L4, L4), L16 = cmul(L8, L8), L32 = cmul(L16, L16), L64 = cmul(L32, L32);
      tab[T_L64 * P_ + p] = L64;
      tab[T_L128 * P_ + p] = cmul(L64, L64);
    }
    unsigned long long* gr = (unsigned long long*)(ws + OFF_GRN);
    for (int k = tid; k < 16 * P_; k += 256) gr[k] = SENT;
  }
}

constexpr int STAGE_B = 40960;
template <int NT>
__device__ __forceinline__ void gemm1_loop(const char* __restrict__ Asrc, const char* __restrict__ Wsrc, char* lds,
                                           f32x4 (&acc)[4][NT / 2], int wid, int lane) {
  constexpr int NQ = (16 + 2 * NT) / 4;
  constexpr int NH = NT / 2;
  const int wm = wid >> 1, wn = wid & 1;
  const char* ag = Asrc + wid * 1024 + lane * 16;
  const char* wg = Wsrc + wid * 1024 + lane * 16;
  char* lw = lds + wid * 1024;
#define G1_STAGE(s) do { _Pragma("unroll") for (int q = 0; q < NQ; ++q) {                                   \
    const char* src = (q < 4) ? (ag + (s) * 16384 + q * 4096) : (wg + (s) * (NT * 2048) + (q - 4) * 4096);   \
    __builtin_amdgcn_global_load_lds((const unsigned*)src, (unsigned*)(lw + ((s) & 3) * STAGE_B + q * 4096), 16, 0, 0); } } while (0)
  const char* la = lds + wm * 8192 + lane * 16;
  const char* lb = lds + 16384 + wn * (NH * 2048) + lane * 16;
#define G1_READ(XA, XW, s, kb2) do {                                                                         \
    _Pragma("unroll") for (int m = 0; m < 4; ++m) XA[m] = *(const bf16x8*)(la + ((s) & 3) * STAGE_B + m * 2048 + (kb2) * 1024);  \
    _Pragma("unroll") for (int n = 0; n < NH; ++n) XW[n] = *(const bf16x8*)(lb + ((s) & 3) * STAGE_B + n * 2048 + (kb2) * 1024); } while (0)
#define G1_MMA(XA, XW) do { _Pragma("unroll") for (int n = 0; n < NH; ++n) _Pragma("unroll") for (int m = 0; m < 4; ++m)  \
    acc[m][n] = __builtin_amdgcn_mfma_f32_16x16x32_bf16(XW[n], XA[m], acc[m][n], 0, 0, 0); } while (0)
  bf16x8 a0[4], w0[NH], a1[4], w1[NH];
  G1_STAGE(0); G1_STAGE(1); G1_STAGE(2);
  WAIT_V(2 * NQ);
  __builtin_amdgcn_s_barrier();
  G1_READ(a0, w0, 0, 0);
#pragma unroll
  for (int s = 0; s < 10; ++s) {
    G1_READ(a1, w1, s, 1);
    SCHED();
    G1_MMA(a0, w0);
    SCHED();
    if (s + 1 < 10) {
      if (s + 2 < 10) WAIT_V(NQ); else WAIT_V(0);
      WAIT_L(0);
      __builtin_amdgcn_s_barrier();
      if (s + 3 < 10) G1_STAGE(s + 3);
      G1_READ(a0, w0, s + 1, 0);
      SCHED();
    }
    G1_MMA(a1, w1);
    SCHED();
  }
#undef G1_STAGE
#undef G1_READ
#undef G1_MMA
}

__global__ __launch_bounds__(256, 1) void kA_kernel(char* __restrict__ ws) {
  __shared__ __attribute__((aligned(1024))) char lds[4 * STAGE_B];
  const int tid = threadIdx.x, wid = __builtin_amdgcn_readfirstlane(tid >> 6), lane = tid & 63, g = lane >> 4, i = lane & 15;
  const int wm = wid >> 1, wn = wid & 1;
  const int xc = blockIdx.x & 7, r = blockIdx.x >> 3;
  const int c = (xc & 1) * 8 + (r & 7);
  const int jq = xc >> 1, jr = r >> 3;
  const bool is_ffn = (jr & 1);
  const int tj = jq * 2 + (jr >> 1);
  const char* Asrc = ws + OFF_XNF + (size_t)c * 163840;
  const int b = 2 * c + wm;
  if (is_ffn) {
    f32x4 acc[4][6] = {};
    gemm1_loop<12>(Asrc, ws + OFF_W1F + (size_t)tj * 245760, lds, acc, wid, lane);
    char* dst = ws + OFF_A1F + ((size_t)b * 48 + 6 * tj + 3 * wn) * 4096 + lane * 16;
#pragma unroll
    for (int q = 0; q < 3; ++q)
#pragma unroll
      for (int m = 0; m < 4; ++m) {
        u32x4 o;
#pragma unroll
        for (int h = 0; h < 2; ++h) {
          f32x4 v = acc[m][2 * q + h];
          float s0 = silu_f(bfr(v[0])), s1 = silu_f(bfr(v[1])), s2 = silu_f(bfr(v[2])), s3 = silu_f(bfr(v[3]));
          o[2 * h] = pack2(s0, s1); o[2 * h + 1] = pack2(s2, s3);
        }
        *(u32x4*)(dst + q * 4096 + m * 1024) = o;
      }
  } else {
    f32x4 acc[4][5] = {};
    gemm1_loop<10>(Asrc, ws + OFF_WBF + (size_t)tj * 204800, lds, acc, wid, lane);
    const f32x4* tab4 = (const f32x4*)(ws + OFF_TAB);
    const f32x2* tab2 = (const f32x2*)(ws + OFF_TAB);
    const int chb = 80 * tj + 40 * wn + 2 * g;
    f32x2 Ep[5][2];
    f32x2 L1[5][2];
    __syncthreads();
    f32x2* endw = (f32x2*)lds;
    f32x2* sin_ = (f32x2*)(lds + 2048);
#pragma unroll
    for (int n = 0; n < 5; ++n) {
      const f32x4 cf4 = tab4[(T_COEF * P_ + chb + 8 * n) >> 1];
      const f32x4 l14 = tab4[(T_L1 * P_ + chb + 8 * n) >> 1];
#pragma unroll
      for (int u = 0; u < 2; ++u) {
        const f32x2 cf = {cf4[2 * u], cf4[2 * u + 1]};
        const f32x2 l1 = {l14[2 * u], l14[2 * u + 1]};
        L1[n][u] = l1;
        f32x2 h = {0.f, 0.f};
#pragma unroll
        for (int m = 0; m < 4; ++m) {
          f32x2 bu = {acc[m][n][2 * u], acc[m][n][2 * u + 1]};
          bu = cmul(cf, bu);
          h = (m == 0) ? bu : cfma(l1, h, bu);
          acc[m][n][2 * u] = h.x; acc[m][n][2 * u + 1] = h.y;
        }
        f32x2 lp = cmul(l1, l1); lp = cmul(lp, lp);
        f32x2 e = h;
        e = cfma(lp, row_shr2<1>(e), e); lp = cmul(lp, lp);
        e = cfma(lp, row_shr2<2>(e), e); lp = cmul(lp, lp);
        e = cfma(lp, row_shr2<4>(e), e); lp = cmul(lp, lp);
        e = cfma(lp, row_shr2<8>(e), e);
        Ep[n][u] = row_shr2<1>(e);
        if (i == 15) endw[wm * 80 + 40 * wn + 8 * n + 2 * g + u] = e;
      }
    }
    __syncthreads();
    if (tid < 80) {
      const int ch = 80 * tj + tid;
      const f32x2 e0 = endw[tid], e1 = endw[80 + tid];
      const f32x2 l64 = tab2[T_L64 * P_ + ch], l128 = tab2[T_L128 * P_ + ch];
      gu64* gr = (gu64*)(ws + OFF_GRN) + ch;
      if (c < 15) {
        f32x2 ec = cfma(l64, e0, e1);
        unsigned long long w = ((unsigned long long)__float_as_uint(ec.y) << 32) | __float_as_uint(ec.x);
        if (w == SENT) w = 0x7FC000007FC00000ull;
        __hip_atomic_store(gr + (size_t)c * P_, w, __ATOMIC_RELAXED, __HIP_MEMORY_SCOPE_AGENT);
      }
      f32x2 S = {0.f, 0.f};
      unsigned long long ev[15];
      for (unsigned spin = 0; spin < (1u << 22); ++spin) {
        bool ok = true;
#pragma unroll
        for (int j = 0; j < 15; ++j) {
          ev[j] = 0;
          if (j < c) { ev[j] = __hip_atomic_load(gr + (size_t)j * P_, __ATOMIC_RELAXED, __HIP_MEMORY_SCOPE_AGENT); ok &= (ev[j] != SENT); }
        }
        if (ok) break;
        __builtin_amdgcn_s_sleep(1);
      }
#pragma unroll
      for (int j = 0; j < 15; ++j)
        if (j < c) { f32x2 e; e.x = __uint_as_float((unsigned)ev[j]); e.y = __uint_as_float((unsigned)(ev[j] >> 32)); S = cfma(l128, S, e); }
      sin_[tid] = S;
      sin_[80 + tid] = cfma(l64, S, e0);
    }
    __syncthreads();
    char* dst = ws + OFF_HF + (size_t)b * 40 * 4096 + lane * 16;
#pragma unroll
    for (int n = 0; n < 5; ++n) {
      const f32x4 p44 = tab4[((T_P4 + i) * P_ + chb + 8 * n) >> 1];
      const f32x4 s4 = *(const f32x4*)(sin_ + wm * 80 + 40 * wn + 8 * n + 2 * g);
#pragma unroll
      for (int u = 0; u < 2; ++u) {
        const f32x2 p4 = {p44[2 * u], p44[2 * u + 1]};
        const f32x2 sw = {s4[2 * u], s4[2 * u + 1]};
        const f32x2 l1 = L1[n][u];
        f32x2 cin = cfma(p4, sw, Ep[n][u]);
#pragma unroll
        for (int m = 0; m < 4; ++m) {
          cin = cmul(l1, cin);
          acc[m][n][2 * u] += cin.x; acc[m][n][2 * u + 1] += cin.y;
        }
      }
      const int NTg = 10 * tj + 5 * wn + n;
#pragma unroll
      for (int m = 0; m < 4; ++m) {
        u32x2 o; o[0] = pack2(acc[m][n][0], acc[m][n][1]); o[1] = pack2(acc[m][n][2], acc[m][n][3]);
        *(u32x2*)(dst + (size_t)(NTg >> 1) * 4096 + m * 1024 + (NTg & 1) * 8) = o;
      }
    }
  }
}

constexpr int KB_D = 7;
__global__ __launch_bounds__(256, 1) void kB_kernel(const char* __restrict__ ws, const unsigned short* __restrict__ x,
                                                    const unsigned short* __restrict__ dskip, float* __restrict__ out) {
  __shared__ __attribute__((aligned(1024))) char lds[2 * 4 * 4 * 5 * 1024];
  const int tid = threadIdx.x, wid = __builtin_amdgcn_readfirstlane(tid >> 6), lane = tid & 63, g = lane >> 4, i = lane & 15;
  const int xc = blockIdx.x & 7, r = blockIdx.x >> 3;
  const int b = (xc >> 1) * 8 + (r & 7), bn = (xc & 1) * 4 + (r >> 3);
  const char* pa = ws + OFF_HF + ((size_t)b * 40 + 10 * wid) * 4096 + lane * 16;
  const char* pw = ws + OFF_CF + ((size_t)bn * 40 + 10 * wid) * 5120 + lane * 16;
  const char* pa2 = ws + OFF_A1F + ((size_t)b * 48 + 12 * wid) * 4096 + lane * 16;
  const char* pw2 = ws + OFF_W2F + ((size_t)bn * 48 + 12 * wid) * 5120 + lane * 16;
  f32x4 acc[2][5][4] = {};
  bf16x8 ra[KB_D][4], rw[KB_D][5];
#define G2_LOAD(s) do {                                                                                      \
    const char* qa = ((s) < 10) ? pa + (s) * 4096 : pa2 + ((s) - 10) * 4096;                                  \
    const char* qw = ((s) < 10) ? pw + (s) * 5120 : pw2 + ((s) - 10) * 5120;                                  \
    _Pragma("unroll") for (int m = 0; m < 4; ++m) ra[(s) % KB_D][m] = *(const bf16x8*)(qa + m * 1024);        \
    _Pragma("unroll") for (int n = 0; n < 5; ++n) rw[(s) % KB_D][n] = *(const bf16x8*)(qw + n * 1024); } while (0)
#pragma unroll
  for (int s = 0; s < KB_D - 1; ++s) G2_LOAD(s);
  SCHED();
#pragma unroll
  for (int s = 0; s < 22; ++s) {
    if (s + KB_D - 1 < 22) G2_LOAD(s + KB_D - 1);
    SCHED();
#pragma unroll
    for (int n = 0; n < 5; ++n)
#pragma unroll
      for (int m = 0; m < 4; ++m)
        acc[s < 10 ? 0 : 1][n][m] = __builtin_amdgcn_mfma_f32_16x16x32_bf16(rw[s % KB_D][n], ra[s % KB_D][m], acc[s < 10 ? 0 : 1][n][m], 0, 0, 0);
    SCHED();
  }
#undef G2_LOAD
#pragma unroll
  for (int a = 0; a < 2; ++a)
#pragma unroll
    for (int m = 0; m < 4; ++m)
      if (m != wid) {
#pragma unroll
        for (int n = 0; n < 5; ++n) *(f32x4*)(lds + (((a * 4 + wid) * 4 + m) * 5 + n) * 1024 + lane * 16) = acc[a][n][m];
      }
  const int t = 64 * b + 4 * i + wid;
  u32x2 xv[5], xnv[5], dv[5];
#pragma unroll
  for (int n = 0; n < 5; ++n) {
    const int d = 80 * bn + 16 * n + 4 * g;
    xv[n] = *(const u32x2*)(x + (size_t)t * D_ + d);
    dv[n] = *(const u32x2*)(dskip + d);
    const int kb = d >> 5, gg = (d & 31) >> 3, j0 = d & 7;
    xnv[n] = *(const u32x2*)(ws + OFF_XNF + (size_t)(b >> 1) * 163840 + (size_t)(kb >> 1) * 16384 +
                             (size_t)((b & 1) * 8 + wid * 2 + (kb & 1)) * 1024 + (gg * 16 + i) * 16 + j0 * 2);
  }
  f32x4 sy[5], so[5];
#pragma unroll
  for (int m = 0; m < 4; ++m)
    if (m == wid) {
#pragma unroll
      for (int n = 0; n < 5; ++n) { sy[n] = acc[0][n][m]; so[n] = acc[1][n][m]; }
    }
  __syncthreads();
#pragma unroll
  for (int sw = 0; sw < 4; ++sw)
    if (sw != wid) {
#pragma unroll
      for (int n = 0; n < 5; ++n) {
        sy[n] += *(const f32x4*)(lds + (((0 * 4 + sw) * 4 + wid) * 5 + n) * 1024 + lane * 16);
        so[n] += *(const f32x4*)(lds + (((1 * 4 + sw) * 4 + wid) * 5 + n) * 1024 + lane * 16);
      }
    }
#pragma unroll
  for (int n = 0; n < 5; ++n) {
    const int d = 80 * bn + 16 * n + 4 * g;
    float xf[4] = {lo_f(xv[n][0]), hi_f(xv[n][0]), lo_f(xv[n][1]), hi_f(xv[n][1])};
    float xnf[4] = {lo_f(xnv[n][0]), hi_f(xnv[n][0]), lo_f(xnv[n][1]), hi_f(xnv[n][1])};
    float df[4] = {lo_f(dv[n][0]), hi_f(dv[n][0]), lo_f(dv[n][1]), hi_f(dv[n][1])};
    f32x4 o;
#pragma unroll
    for (int e = 0; e < 4; ++e) {
      const float gt = bfr(silu_f(bfr(so[n][e])));
      const float y = 2.f * sy[n][e] + bfr(xnf[e] * df[e]);
      o[e] = xf[e] + y * gt;
    }
    *(f32x4*)(out + (size_t)t * D_ + d) = o;
  }
}

extern "C" void kernel_launch(void* const* d_in, const int* in_sizes, int n_in, void* d_out, int out_size, void* d_ws,
                              size_t ws_size, hipStream_t stream) {
  const unsigned short* x = (const unsigned short*)d_in[0];
  const unsigned short* lre = (const unsigned short*)d_in[1];
  const unsigned short* lim = (const unsigned short*)d_in[2];
  const unsigned short* Bre = (const unsigned short*)d_in[3];
  const unsigned short* Bim = (const unsigned short*)d_in[4];
  const unsigned short* Cre = (const unsigned short*)d_in[5];
  const unsigned short* Cim = (const unsigned short*)d_in[6];
  const unsigned short* logdt = (const unsigned short*)d_in[7];
  const unsigned short* dskip = (const unsigned short*)d_in[8];
  const unsigned short* w1 = (const unsigned short*)d_in[10];
  const unsigned short* w2 = (const unsigned short*)d_in[11];
  char* ws = (char*)d_ws;
  prep_kernel<<<PREP_LN + PREP_PACK + 1, 256, 0, stream>>>(x, lre, lim, Bre, Bim, Cre, Cim, logdt, w1, w2, ws);
  kA_kernel<<<256, 256, 0, stream>>>(ws);
  kB_kernel<<<256, 256, 0, stream>>>(ws, x, dskip, (float*)d_out);
}
```

```cpp
#include <hip/hip_runtime.h>
#include <hip/hip_bf16.h>
#include <stdint.h>

typedef __attribute__((ext_vector_type(8))) short bf16x8;
typedef __attribute__((ext_vector_type(4))) short s16x4;
typedef __attribute__((ext_vector_type(4))) float f32x4;
typedef __attribute__((ext_vector_type(2))) float f32x2;
typedef __attribute__((ext_vector_type(4))) unsigned u32x4;
typedef __attribute__((ext_vector_type(2))) unsigned u32x2;
typedef __attribute__((ext_vector_type(2))) __bf16 bf2_t;
#define GAS __attribute__((address_space(1)))
#define LAS __attribute__((address_space(3)))
typedef LAS char* lptr;
typedef const GAS char* gptr;

#define WAIT_V(n) asm volatile("s_waitcnt vmcnt(%0)" ::"n"(n) : "memory")
#define WAIT_L(n) asm volatile("s_waitcnt lgkmcnt(%0)" ::"n"(n) : "memory")
#define SCHED() __builtin_amdgcn_sched_barrier(0)
#define MEMBAR() asm volatile("" ::: "memory")
#define BAR() do { MEMBAR(); __builtin_amdgcn_s_barrier(); MEMBAR(); } while (0)

constexpr int W_EPOCH = 0;
constexpr int W_FLAGA = 4096;
constexpr int W_FLAGH = 8192;
constexpr int W_STAT  = 16384;
constexpr int W_CSB   = 49152;
constexpr int W_CSF   = 65536;
constexpr int W_EGR   = 196608;
constexpr int W_AF    = 393216;
constexpr int W_GB    = W_AF + 16 * 8 * 88 * 1024;
constexpr int W_END   = W_GB + 16 * 4 * 40 * 2048;
constexpr int W_DBG   = W_END;
constexpr int SLOT1 = 40960;
constexpr int MISC = 143360;
constexpr int M_STAT = MISC;
constexpr int M_CS   = MISC + 1024;
constexpr int M_TAB  = MISC + 2048;
constexpr int M_P4   = MISC + 4608;
constexpr int M_ENDW = MISC + 11264;
constexpr int M_SIN  = MISC + 12032;
constexpr int M_SYNC = MISC + 12800;
constexpr unsigned SPIN_CAP = 1u << 16;

__device__ __forceinline__ float bf2f(unsigned short v) { return __uint_as_float((unsigned)v << 16); }
__device__ __forceinline__ float bfr(float a) { return (float)(__bf16)a; }
__device__ __forceinline__ unsigned pack2(float a, float b) {
  bf2_t v = {(__bf16)a, (__bf16)b};
  return __builtin_bit_cast(unsigned, v);
}
__device__ __forceinline__ float lo_f(unsigned u) { return __uint_as_float(u << 16); }
__device__ __forceinline__ float hi_f(unsigned u) { return __uint_as_float(u & 0xFFFF0000u); }
__device__ __forceinline__ float silu_f(float x) { return x * __builtin_amdgcn_rcpf(1.f + __expf(-x)); }
__device__ __forceinline__ f32x2 cmul(f32x2 a, f32x2 b) {
  f32x2 r; r.x = a.x * b.x - a.y * b.y; r.y = a.x * b.y + a.y * b.x; return r;
}
__device__ __forceinline__ f32x2 cfma(f32x2 a, f32x2 b, f32x2 c) {
  f32x2 r; r.x = fmaf(a.x, b.x, fmaf(-a.y, b.y, c.x)); r.y = fmaf(a.x, b.y, fmaf(a.y, b.x, c.y)); return r;
}
template <int N> __device__ __forceinline__ float row_shr(float v) {
  return __builtin_bit_cast(float, __builtin_amdgcn_update_dpp(0, __builtin_bit_cast(int, v), 0x110 + N, 0xf, 0xf, true));
}
template <int N> __device__ __forceinline__ f32x2 row_shr2(f32x2 v) { f32x2 r; r.x = row_shr<N>(v.x); r.y = row_shr<N>(v.y); return r; }
__device__ __forceinline__ float sum8(u32x4 v) {
  return ((lo_f(v[0]) + hi_f(v[0])) + (lo_f(v[1]) + hi_f(v[1]))) + ((lo_f(v[2]) + hi_f(v[2])) + (lo_f(v[3]) + hi_f(v[3])));
}
__device__ __forceinline__ void glds(gptr g, lptr l) {
  __builtin_amdgcn_global_load_lds((const GAS unsigned*)g, (LAS unsigned*)l, 16, 0, 0);
}
__device__ __forceinline__ bf16x8 lds_b128(lptr p) { return *(const LAS bf16x8*)p; }
__device__ __forceinline__ bf16x8 lds_tr2(lptr p0, lptr p1) {
  s16x4 a = __builtin_amdgcn_ds_read_tr16_b64_v4i16((LAS s16x4*)p0);
  s16x4 b = __builtin_amdgcn_ds_read_tr16_b64_v4i16((LAS s16x4*)p1);
  return __builtin_shufflevector(a, b, 0, 1, 2, 3, 4, 5, 6, 7);
}
__device__ __forceinline__ bf16x8 lds_2b64(lptr p0, lptr p1) {
  s16x4 a = *(const LAS s16x4*)p0;
  s16x4 b = *(const LAS s16x4*)p1;
  return __builtin_shufflevector(a, b, 0, 1, 2, 3, 4, 5, 6, 7);
}
__device__ __forceinline__ lptr opaque(lptr p) {
  unsigned v = (unsigned)(size_t)p;
  asm volatile("" : "+v"(v));
  return (lptr)(size_t)v;
}
typedef __amdgpu_buffer_rsrc_t rsrc_t;
__device__ __forceinline__ u32x4 ld_sc1(rsrc_t r, int voff, int soff) { return __builtin_amdgcn_raw_buffer_load_b128(r, voff, soff, 16); }
__device__ __forceinline__ void st_sc1(u32x4 v, rsrc_t r, int voff, int soff) { __builtin_amdgcn_raw_buffer_store_b128(v, r, voff + soff, 0, 16); }
__device__ __forceinline__ unsigned lds_ld(lptr p) { return *(volatile LAS unsigned*)p; }
__device__ __forceinline__ void lds_spin(lptr p, unsigned want) {
  for (unsigned s = 0; s < (1u << 22); ++s) {
    if (__builtin_amdgcn_readfirstlane(lds_ld(p)) == want) break;
    __builtin_amdgcn_s_sleep(1);
  }
  MEMBAR();
}
__device__ __forceinline__ unsigned lds_inc(lptr p) {
  unsigned r = 0;
  if ((threadIdx.x & 63) == 0) r = __hip_atomic_fetch_add((LAS unsigned*)p, 1u, __ATOMIC_RELAXED, __HIP_MEMORY_SCOPE_WORKGROUP);
  return __builtin_amdgcn_readfirstlane(r);
}
#define TS(k) do {} while (0)

struct Params {
  const char *x, *lre, *lim, *Bre, *Bim, *Cre, *Cim, *logdt, *dskip, *w1, *w2;
  char* ws;
  float* out;
};
struct Geo {
  int c, type, a, pid, par, u, jj;
};

template <int NB, int NF>
__device__ __forceinline__ void p1_mfma(const Params& P, const Geo& G, lptr lds, rsrc_t rs, int wid, int lane, int pair0, int foff,
                                        int fstep0, int hstep0, int csf0) {
  constexpr int NT = 2 * NB + 2 * NF;
  const int g = lane >> 4, i = lane & 15, wm = wid >> 1;
  f32x4 acc[4][NT];
#pragma unroll
  for (int m = 0; m < 4; ++m)
#pragma unroll
    for (int n = 0; n < NT; ++n) acc[m][n] = (f32x4){0.f, 0.f, 0.f, 0.f};
  const int sw = (g ^ (i & 7)) * 16;
  lptr bA[2][2], bB[2][2], bF[2][4];
  bA[0][0] = lds + wm * 8192 + i * 128 + sw; bA[0][1] = lds + wm * 8192 + i * 128 + (sw ^ 64);
  bB[0][0] = lds + 16384 + pair0 * 4096 + i * 128 + sw; bB[0][1] = lds + 16384 + pair0 * 4096 + i * 128 + (sw ^ 64);
  const int sg = ((i >> 3) & 1) | ((g & 1) << 1);
#pragma unroll
  for (int n = 0; n < 4; ++n) bF[0][n] = lds + foff + (8 * g + (i >> 2)) * 128 + ((n ^ sg) * 32) + 8 * (i & 3);
#pragma unroll
  for (int k = 0; k < 2; ++k) { bA[1][k] = opaque(bA[0][k] + 2 * SLOT1); bB[1][k] = opaque(bB[0][k] + 2 * SLOT1); }
#pragma unroll
  for (int n = 0; n < 2 * NF; ++n) bF[1][n] = opaque(bF[0][n] + 2 * SLOT1);
#define P1_READ(XA, XW, s, kb) do {                                                                              \
    _Pragma("unroll") for (int m = 0; m < 4; ++m) XA[m] = lds_b128(bA[((s) >> 1) & 1][kb] + ((s) & 1) * SLOT1 + m * 2048);   \
    _Pragma("unroll") for (int n = 0; n < 2 * NB; ++n) XW[n] = lds_b128(bB[((s) >> 1) & 1][kb] + ((s) & 1) * SLOT1 + n * 2048);  \
    _Pragma("unroll") for (int n = 0; n < 2 * NF; ++n) {                                                          \
      lptr q_ = bF[((s) >> 1) & 1][n] + ((s) & 1) * SLOT1 + (kb) * 4096; XW[2 * NB + n] = lds_tr2(q_, q_ + 512); } } while (0)
#define P1_MMA(XA, XW) do { _Pragma("unroll") for (int n = 0; n < NT; ++n) _Pragma("unroll") for (int m = 0; m < 4; ++m)  \
    acc[m][n] = __builtin_amdgcn_mfma_f32_16x16x32_bf16(XW[n], XA[m], acc[m][n], 0, 0, 0); } while (0)
  bf16x8 a0[4], w0[NT], a1[4], w1[NT];
  BAR();
  TS(1);
  P1_READ(a0, w0, 0, 0);
#pragma unroll
  for (int s = 0; s < 10; ++s) {
    P1_READ(a1, w1, s, 1);
    SCHED();
    P1_MMA(a0, w0);
    SCHED();
    WAIT_L(0);
    BAR();
    if (s + 1 < 10) { P1_READ(a0, w0, s + 1, 0); SCHED(); }
    P1_MMA(a1, w1);
    SCHED();
  }
#undef P1_READ
#undef P1_MMA
  TS(2);
  float mb[4], rd[4];
  {
    const f32x4 s0 = *(const LAS f32x4*)(lds + M_STAT + (64 * wm + 4 * i) * 8);
    const f32x4 s1 = *(const LAS f32x4*)(lds + M_STAT + (64 * wm + 4 * i) * 8 + 16);
    mb[0] = s0[0]; mb[1] = s0[2]; mb[2] = s1[0]; mb[3] = s1[2];
    rd[0] = 1.f / s0[1]; rd[1] = 1.f / s0[3]; rd[2] = 1.f / s1[1]; rd[3] = 1.f / s1[3];
  }
  f32x2 Ep[NB][4];
#pragma unroll
  for (int p = 0; p < NB; ++p) {
    const int chl = 16 * (pair0 + p) + 4 * g;
    lptr tb = lds + M_TAB + chl * 8;
#pragma unroll
    for (int a2 = 0; a2 < 2; ++a2) {
      const int n = 2 * p + a2;
      const f32x4 cs = *(const LAS f32x4*)(lds + M_CS + ((2 * (pair0 + p) + a2) * 16 + 4 * g) * 4);
      const f32x4 l1 = *(const LAS f32x4*)(tb + a2 * 16);
      const f32x4 l4 = *(const LAS f32x4*)(tb + 384 + a2 * 16);
      const f32x4 l8 = *(const LAS f32x4*)(tb + 768 + a2 * 16);
      const f32x4 l16 = *(const LAS f32x4*)(tb + 1152 + a2 * 16);
      const f32x4 l32 = *(const LAS f32x4*)(tb + 1536 + a2 * 16);
#pragma unroll
      for (int u = 0; u < 2; ++u) {
        const f32x2 L1 = {l1[2 * u], l1[2 * u + 1]};
        f32x2 h;
#pragma unroll
        for (int m = 0; m < 4; ++m) {
          f32x2 bu;
          bu.x = (acc[m][n][2 * u] - mb[m] * cs[2 * u]) * rd[m];
          bu.y = (acc[m][n][2 * u + 1] - mb[m] * cs[2 * u + 1]) * rd[m];
          h = (m == 0) ? bu : cfma(L1, h, bu);
          acc[m][n][2 * u] = h.x; acc[m][n][2 * u + 1] = h.y;
        }
        f32x2 e = h;
        e = cfma((f32x2){l4[2 * u], l4[2 * u + 1]}, row_shr2<1>(e), e);
        e = cfma((f32x2){l8[2 * u], l8[2 * u + 1]}, row_shr2<2>(e), e);
        e = cfma((f32x2){l16[2 * u], l16[2 * u + 1]}, row_shr2<4>(e), e);
        e = cfma((f32x2){l32[2 * u], l32[2 * u + 1]}, row_shr2<8>(e), e);
        Ep[p][2 * a2 + u] = row_shr2<1>(e);
        if (i == 15) *(LAS f32x2*)(lds + M_ENDW + (wm * 48 + chl + 2 * a2 + u) * 8) = e;
      }
    }
  }
  WAIT_L(0);
  lds_inc(lds + M_SYNC);
  const int afv = W_AF + (G.c * 8 + 4 * wm) * 90112 + lane * 16;
  const unsigned tag = lds_ld(lds + M_SYNC + 20);
  if (NF > 0) {
#pragma unroll
    for (int f = 0; f < NF; ++f) {
      f32x4 cs[2];
      cs[0] = *(const LAS f32x4*)(lds + M_CS + (csf0 + 32 * f + 4 * g) * 4);
      cs[1] = *(const LAS f32x4*)(lds + M_CS + (csf0 + 32 * f + 16 + 4 * g) * 4);
#pragma unroll
      for (int m = 0; m < 4; ++m) {
        u32x4 o;
#pragma unroll
        for (int h = 0; h < 2; ++h) {
          const f32x4 v = acc[m][2 * NB + 2 * f + h];
          float s[4];
#pragma unroll
          for (int e = 0; e < 4; ++e) s[e] = silu_f(bfr((v[e] - mb[m] * cs[h][e]) * rd[m]));
          o[2 * h] = pack2(s[0], s[1]); o[2 * h + 1] = pack2(s[2], s[3]);
        }
        st_sc1(o, rs, afv + m * 90112 + f * 1024, fstep0 * 1024);
      }
    }
    WAIT_V(0);
  }
  if (lds_inc(lds + M_SYNC + 4) == 3 && lane == 0) {
    u32x2 fl = {tag, ~tag};
    __builtin_amdgcn_raw_buffer_store_b64(fl, rs, W_FLAGA + (G.c * 16 + G.pid) * 8, 0, 16);
  }
  TS(3);
  lds_spin(lds + M_SYNC + 12, 1u);
  TS(4);
#pragma unroll
  for (int p = 0; p < NB; ++p) {
    const int chl = 16 * (pair0 + p) + 4 * g;
    lptr tb = lds + M_TAB + chl * 8;
    float hr[4][4], hi[4][4];
#pragma unroll
    for (int a2 = 0; a2 < 2; ++a2) {
      const int n = 2 * p + a2;
      const f32x4 l1 = *(const LAS f32x4*)(tb + a2 * 16);
      const f32x4 cf = *(const LAS f32x4*)(tb + 1920 + a2 * 16);
      const f32x4 p4 = *(const LAS f32x4*)(lds + M_P4 + i * 400 + chl * 8 + a2 * 16);
      const f32x4 sn = *(const LAS f32x4*)(lds + M_SIN + (wm * 48 + chl) * 8 + a2 * 16);
#pragma unroll
      for (int u = 0; u < 2; ++u) {
        const f32x2 L1 = {l1[2 * u], l1[2 * u + 1]};
        const f32x2 CF = {cf[2 * u], cf[2 * u + 1]};
        f32x2 cin = cfma((f32x2){p4[2 * u], p4[2 * u + 1]}, (f32x2){sn[2 * u], sn[2 * u + 1]}, Ep[p][2 * a2 + u]);
#pragma unroll
        for (int m = 0; m < 4; ++m) {
          cin = cmul(L1, cin);
          f32x2 hh = {acc[m][n][2 * u] + cin.x, acc[m][n][2 * u + 1] + cin.y};
          hh = cmul(CF, hh);
          hr[m][2 * a2 + u] = hh.x; hi[m][2 * a2 + u] = -hh.y;
        }
      }
    }
#pragma unroll
    for (int m = 0; m < 4; ++m) {
      u32x4 o;
      o[0] = pack2(hr[m][0], hr[m][1]); o[1] = pack2(hr[m][2], hr[m][3]);
      o[2] = pack2(hi[m][0], hi[m][1]); o[3] = pack2(hi[m][2], hi[m][3]);
      st_sc1(o, rs, afv + m * 90112 + p * 1024, hstep0 * 1024);
    }
  }
  WAIT_V(0);
  if (lds_inc(lds + M_SYNC + 8) == 3 && lane == 0) {
    u32x2 fl = {tag, ~tag};
    __builtin_amdgcn_raw_buffer_store_b64(fl, rs, W_FLAGH + (G.c * 16 + G.pid) * 8, 0, 16);
  }
  TS(5);
}

constexpr int DO = 16, DY = 12;
__device__ __forceinline__ void p2_o(const Params& P, const Geo& G, lptr lds, rsrc_t rs, int wid, int lane, int ct, int q0) {
  const int g = lane >> 4, i = lane & 15;
  f32x4 acc[2][4];
#pragma unroll
  for (int m = 0; m < 2; ++m)
#pragma unroll
    for (int n = 0; n < 4; ++n) acc[m][n] = (f32x4){0.f, 0.f, 0.f, 0.f};
  const int sg = 2 * (g & 1) + (i >> 3);
  lptr bW[2][4];
#pragma unroll
  for (int n = 0; n < 4; ++n) { bW[0][n] = lds + (4 * g + (i >> 2)) * 128 + ((n ^ sg) * 32) + 8 * (i & 3); bW[1][n] = opaque(bW[0][n] + 65536); }
  const int v0 = W_AF + (G.c * 8 + 2 * wid) * 90112 + lane * 16, v1 = v0 + 90112;
  const unsigned tag = lds_ld(lds + M_SYNC + 20);
  lds_spin(lds + M_SYNC + 16, 1u);
  __builtin_amdgcn_s_waitcnt(0x0F70);
  TS(6);
  u32x4 ra[DO][2];
  bf16x8 wf[2][4];
#define O_SOFF(s) ((((((s) >> 2) + q0) >= 12) ? ((s) >> 2) + q0 - 12 : ((s) >> 2) + q0) * 4096)
#define O_LOAD(s) do { ra[(s) % DO][0] = ld_sc1(rs, v0 + ((s) & 3) * 1024, O_SOFF(s)); ra[(s) % DO][1] = ld_sc1(rs, v1 + ((s) & 3) * 1024, O_SOFF(s)); } while (0)
#define O_READ(s) do { _Pragma("unroll") for (int n = 0; n < 4; ++n) {                                             \
      lptr q_ = bW[((((s) >> 2) + 5) >> 2) & 1][n] + ((((s) >> 2) + 5) & 3) * 16384 + ((s) & 3) * 4096; wf[(s) & 1][n] = lds_tr2(q_, q_ + 2048); } } while (0)
#pragma unroll
  for (int s = 0; s < DO; ++s) O_LOAD(s);
  SCHED();
  BAR();
  O_READ(0);
#pragma unroll
  for (int s = 0; s < 48; ++s) {
    if (s + 1 < 48) {
      if (((s + 1) & 3) == 0) { WAIT_L(0); BAR(); }
      O_READ(s + 1);
    }
    SCHED();
#pragma unroll
    for (int n = 0; n < 4; ++n)
#pragma unroll
      for (int m = 0; m < 2; ++m)
        acc[m][n] = __builtin_amdgcn_mfma_f32_16x16x32_bf16(wf[s & 1][n], __builtin_bit_cast(bf16x8, ra[s % DO][m]), acc[m][n], 0, 0, 0);
    SCHED();
    if (s + DO < 48) O_LOAD(s + DO);
    SCHED();
  }
#undef O_LOAD
#undef O_READ
#undef O_SOFF
  TS(7);
  const int gv = W_GB + ((G.c * 4 + wid) * 40 + 4 * ct) * 2048 + lane * 16;
#pragma unroll
  for (int n = 0; n < 4; ++n)
#pragma unroll
    for (int m = 0; m < 2; ++m) {
      float s[4];
#pragma unroll
      for (int e = 0; e < 4; ++e) s[e] = silu_f(bfr(acc[m][n][e]));
      u32x4 o = {pack2(s[0], s[1]), tag, pack2(s[2], s[3]), tag};
      st_sc1(o, rs, gv + n * 2048 + m * 1024, 0);
    }
  TS(8);
}

__device__ __forceinline__ void p2_y(const Params& P, const Geo& G, lptr lds, rsrc_t rs, int wid, int lane, int n0, int skip0, int q0) {
  const int g = lane >> 4, i = lane & 15;
  const int tl = 64 * (wid >> 1) + 4 * i + 2 * (wid & 1);
  const GAS char* xrow = (const GAS char*)P.x + (size_t)(128 * G.c + tl) * 1280 + (16 * n0 + 4 * g) * 2;
  f32x4 acc[2][7];
  {
    const f32x4 st = *(const LAS f32x4*)(lds + M_STAT + tl * 8);
#pragma unroll
    for (int n = 0; n < 7; ++n) {
      const u32x2 dv = *(const GAS u32x2*)((const GAS char*)P.dskip + (16 * (n0 + n) + 4 * g) * 2);
      const float df[4] = {lo_f(dv[0]), hi_f(dv[0]), lo_f(dv[1]), hi_f(dv[1])};
#pragma unroll
      for (int m = 0; m < 2; ++m) {
        const u32x2 xv = *(const GAS u32x2*)(xrow + m * 1280 + n * 32);
        const float xf[4] = {lo_f(xv[0]), hi_f(xv[0]), lo_f(xv[1]), hi_f(xv[1])};
#pragma unroll
        for (int e = 0; e < 4; ++e) {
          const float xn = bfr(bfr(xf[e] - st[2 * m]) / st[2 * m + 1]);
          acc[m][n][e] = 0.5f * bfr(xn * df[e]);
        }
      }
    }
  }
  lptr bC[3][4];
#pragma unroll
  for (int s = 0; s < 4; ++s) {
    bC[0][s] = lds + i * 128 + (((2 * s + (g >> 1)) ^ ((i >> 1) & 7)) * 16) + 8 * (g & 1);
    bC[1][s] = opaque(bC[0][s] + 57344); bC[2][s] = opaque(bC[0][s] + 114688);
  }
  const int v0 = W_AF + (G.c * 8 + 2 * wid) * 90112 + 48 * 1024 + lane * 16, v1 = v0 + 90112;
  const unsigned tag = lds_ld(lds + M_SYNC + 20);
  lds_spin(lds + M_SYNC + 16, 1u);
  TS(6);
  u32x4 ra[DY][2];
  bf16x8 wf[2][7];
#define Y_SOFF(s) ((((((s) >> 2) + q0) >= 10) ? ((s) >> 2) + q0 - 10 : ((s) >> 2) + q0) * 4096)
#define Y_LOAD(s) do { ra[(s) % DY][0] = ld_sc1(rs, v0 + ((s) & 3) * 1024, Y_SOFF(s)); ra[(s) % DY][1] = ld_sc1(rs, v1 + ((s) & 3) * 1024, Y_SOFF(s)); } while (0)
#define Y_READ(s) do { _Pragma("unroll") for (int n = 0; n < 7; ++n) {                                             \
      lptr q_ = bC[((((s) >> 2) + 3) % 5) >> 1][(s) & 3] + (((((s) >> 2) + 3) % 5) & 1) * 28672 + n * 2048; wf[(s) & 1][n] = lds_2b64(q_, q_ + 14336); } } while (0)
  const int gv = W_GB + ((G.c * 4 + wid) * 40 + n0) * 2048 + lane * 16;
  u32x4 gq[7][2];
  u32x2 xq[7][2];
#pragma unroll
  for (int s = 0; s < DY; ++s) Y_LOAD(s);
  SCHED();
  BAR();
  Y_READ(0);
#pragma unroll
  for (int s = 0; s < 40; ++s) {
    if (s + 1 < 40) {
      if (((s + 1) & 3) == 0) { WAIT_L(0); BAR(); }
      Y_READ(s + 1);
    }
    SCHED();
#pragma unroll
    for (int n = 0; n < 7; ++n)
#pragma unroll
      for (int m = 0; m < 2; ++m)
        acc[m][n] = __builtin_amdgcn_mfma_f32_16x16x32_bf16(wf[s & 1][n], __builtin_bit_cast(bf16x8, ra[s % DY][m]), acc[m][n], 0, 0, 0);
    SCHED();
    if (s + DY < 40) Y_LOAD(s + DY);
    else if (s >= 30 && s < 37) {
      const int n = s - 30;
      xq[n][0] = *(const GAS u32x2*)(xrow + n * 32); xq[n][1] = *(const GAS u32x2*)(xrow + 1280 + n * 32);
      gq[n][0] = ld_sc1(rs, gv + n * 2048, 0); gq[n][1] = ld_sc1(rs, gv + n * 2048 + 1024, 0);
    }
    SCHED();
  }
#undef Y_LOAD
#undef Y_READ
#undef Y_SOFF
  TS(7);
  for (unsigned sp = 0; sp < SPIN_CAP; ++sp) {
    bool ok = true;
#pragma unroll
    for (int n = 0; n < 7; ++n)
#pragma unroll
      for (int m = 0; m < 2; ++m) ok &= (gq[n][m][1] == tag) & (gq[n][m][3] == tag);
    if (__all(ok)) break;
    __builtin_amdgcn_s_sleep(2);
    MEMBAR();
#pragma unroll
    for (int n = 0; n < 7; ++n) { gq[n][0] = ld_sc1(rs, gv + n * 2048, 0); gq[n][1] = ld_sc1(rs, gv + n * 2048 + 1024, 0); }
  }
  GAS char* orow = (GAS char*)P.out + (size_t)(128 * G.c + tl) * 2560 + (16 * n0 + 4 * g) * 4;
#pragma unroll
  for (int n = 0; n < 7; ++n) {
    if (n == 0 && skip0) continue;
#pragma unroll
    for (int m = 0; m < 2; ++m) {
      const float xf[4] = {lo_f(xq[n][m][0]), hi_f(xq[n][m][0]), lo_f(xq[n][m][1]), hi_f(xq[n][m][1])};
      const float gf[4] = {lo_f(gq[n][m][0]), hi_f(gq[n][m][0]), lo_f(gq[n][m][2]), hi_f(gq[n][m][2])};
      f32x4 o;
#pragma unroll
      for (int e = 0; e < 4; ++e) o[e] = xf[e] + (2.f * acc[m][n][e]) * gf[e];
      *(GAS f32x4*)(orow + m * 2560 + n * 64) = o;
    }
  }
  TS(8);
}

template <int TYPE, int ROLE>
__device__ __forceinline__ void loader(const Params& P, const Geo& G, lptr lds, rsrc_t rs, int H, int lane, int t2, int q0) {
  constexpr int NBP = TYPE ? 4 : 6, NFP = TYPE ? 8 : 4, NQ = 8 + NBP + NFP;
  constexpr int FOFF = TYPE ? 24576 : 28672;
  const int r8 = lane >> 3, c8 = lane & 7;
  const int chb = TYPE ? 384 + 32 * G.a : 48 * G.a;
  const int colb = TYPE ? 512 + 128 * G.a : 64 * G.a;
  gptr pA = (gptr)P.x + (size_t)(128 * G.c + 4 * (8 * H + r8)) * 1280 + ((c8 ^ r8) * 16);
  gptr pB = (gptr)((r8 & 1) ? P.Bim : P.Bre) + (size_t)(chb + 4 * (2 * H + (r8 >> 2)) + ((r8 >> 1) & 1)) * 1280 + ((c8 ^ r8) * 16);
  const int sgF = ((r8 >> 1) & 1) | (H << 1);
  gptr pF = (gptr)P.w1 + (size_t)(8 * H + r8) * 3072 + colb * 2 + ((((c8 >> 1) ^ sgF) << 1) | (c8 & 1)) * 16;
  lptr lw = lds + H * 1024;
#define L1_STAGE(s) do {                                                                                          \
    _Pragma("unroll") for (int q = 0; q < 8; ++q)                                                                  \
      glds(pA + (64 * (q >> 2) + (q & 3)) * 1280 + 128 * (s), lw + ((s) & 3) * SLOT1 + q * 2048);                  \
    _Pragma("unroll") for (int q = 0; q < NBP; ++q)                                                                \
      glds(pB + (16 * (q >> 1) + 2 * (q & 1)) * 1280 + 128 * (s), lw + ((s) & 3) * SLOT1 + 16384 + q * 2048);      \
    _Pragma("unroll") for (int q = 0; q < NFP; ++q)                                                                \
      glds(pF + ((s) * 64 + (q & 3) * 16) * 3072 + (q >> 2) * 128, lw + ((s) & 3) * SLOT1 + FOFF + q * 2048); } while (0)
  constexpr int NQ2 = ROLE ? 14 : 8, NS2 = ROLE ? 10 : 12, R2 = ROLE ? 5 : 8;
  gptr p2a, p2b;
  if (ROLE == 0) {
    p2a = (gptr)P.w2 + (size_t)(64 * H + r8) * 1280 + t2 * 128 + ((((c8 >> 1) ^ ((r8 >> 1) & 3)) << 1) | (c8 & 1)) * 16;
    p2b = p2a;
  } else {
    gptr cb = (gptr)(H ? P.Cim : P.Cre) + (size_t)(16 * t2 + r8) * 1280;
    p2a = cb + ((c8 ^ (r8 >> 1)) * 16);
    p2b = cb + 8 * 1280 + ((c8 ^ (4 + (r8 >> 1))) * 16);
  }
#define L2_STAGE(q) do {                                                                                          \
    const int qq_ = ((q) + q0 >= NS2) ? (q) + q0 - NS2 : (q) + q0;                                                 \
    if (ROLE == 0) { _Pragma("unroll") for (int p = 0; p < 8; ++p)                                                 \
        glds(p2a + (size_t)qq_ * (128 * 1280) + p * (8 * 1280), lds + (((q) + 5) & 7) * 16384 + H * 8192 + p * 1024); }  \
    else { _Pragma("unroll") for (int p = 0; p < 14; ++p)                                                          \
        glds(((p & 1) ? p2b : p2a) + qq_ * 128 + (p >> 1) * (16 * 1280), lds + (((q) + 3) % 5) * 28672 + H * 14336 + p * 1024); } } while (0)
  L1_STAGE(0); L1_STAGE(1); L1_STAGE(2);
  constexpr int I7 = ROLE ? 1 : 2, I8 = 1, I9 = ROLE ? 1 : 2;
#pragma unroll
  for (int s = 0; s < 10; ++s) {
    if (s <= 7) WAIT_V(2 * NQ);
    else if (s == 8) WAIT_V(NQ + I7 * NQ2);
    else WAIT_V((I7 + I8) * NQ2);
    BAR();
    if (s + 3 < 10) L1_STAGE(s + 3);
    if (s == 7) { L2_STAGE(0); if (I7 == 2) L2_STAGE(1); }
    if (s == 8) { L2_STAGE(I7); }
    if (s == 9) { L2_STAGE(I7 + I8); if (I9 == 2) L2_STAGE(I7 + I8 + 1); }
  }
  BAR();
  WAIT_V((I8 + I9) * NQ2);
#pragma unroll
  for (int q = I7 + I8 + I9; q < R2; ++q) L2_STAGE(q);
#pragma unroll
  for (int q = 0; q < NS2; ++q) {
    const int last = (q == 0) ? R2 - 1 : ((q + R2 - 2 < NS2 - 1) ? q + R2 - 2 : NS2 - 1);
    const int n = (last - q) * NQ2;
    if (n >= 63) WAIT_V(63); else if (n == 56) WAIT_V(56); else if (n == 48) WAIT_V(48); else if (n == 42) WAIT_V(42);
    else if (n == 40) WAIT_V(40); else if (n == 32) WAIT_V(32); else if (n == 28) WAIT_V(28); else if (n == 24) WAIT_V(24);
    else if (n == 16) WAIT_V(16); else if (n == 14) WAIT_V(14); else if (n == 8) WAIT_V(8); else WAIT_V(0);
    BAR();
    if (q >= 1 && q - 1 + R2 < NS2) L2_STAGE(q - 1 + R2);
  }
#undef L1_STAGE
#undef L2_STAGE
}

__device__ __forceinline__ unsigned read_tag(rsrc_t rs) {
  return __builtin_amdgcn_readfirstlane(__builtin_amdgcn_raw_buffer_load_b32(rs, W_EPOCH, 0, 16)) + 1u;
}
__device__ __forceinline__ void service_h2(const Params& P, const Geo& G, lptr lds, rsrc_t rs, int lane, int role) {
  const int r8 = lane >> 3, c8 = lane & 7;
  const unsigned tag = read_tag(rs);
  u32x4 v[10];
  {
    const GAS char* src = (const GAS char*)P.x + (size_t)(128 * G.c + 8 * G.pid + r8) * 1280 + c8 * 16;
#pragma unroll
    for (int q = 0; q < 10; ++q) v[q] = *(const GAS u32x4*)(src + q * 128);
  }
  BAR();
  {
    float s = 0.f;
#pragma unroll
    for (int q = 0; q < 10; ++q) s += sum8(v[q]);
    s += __shfl_xor(s, 1); s += __shfl_xor(s, 2); s += __shfl_xor(s, 4);
    const float mean = s * (1.f / 640.f);
    float ss = 0.f;
#pragma unroll
    for (int q = 0; q < 10; ++q)
#pragma unroll
      for (int e = 0; e < 4; ++e) { const float a = lo_f(v[q][e]) - mean, b = hi_f(v[q][e]) - mean; ss += a * a + b * b; }
    ss += __shfl_xor(ss, 1); ss += __shfl_xor(ss, 2); ss += __shfl_xor(ss, 4);
    const float mb = bfr(mean);
    const float den = bfr(sqrtf(bfr(bfr(ss * (1.f / 640.f)) + 1e-5f)));
    if (c8 == 0) {
      u32x4 o = {__float_as_uint(mb), tag, __float_as_uint(den), tag};
      st_sc1(o, rs, W_STAT + (128 * G.c + 8 * G.pid + r8) * 16, 0);
    }
  }
  BAR();
  BAR();
  BAR();
  BAR();
  const int nch = G.type ? 32 : 48, chb = G.type ? 384 + 32 * G.a : 48 * G.a;
  const int ncol = G.type ? 128 : 64, colb = G.type ? 512 + 128 * G.a : 64 * G.a;
  const int vs = W_STAT + (128 * G.c + lane) * 16;
  const int vb = W_CSB + (chb + (lane < nch ? lane : 0)) * 16;
  const int vf = W_CSF + (colb + (2 * lane < ncol ? 2 * lane : 0)) * 8;
  u32x4 s0, s1, cb, cf[10];
#define H2_GATHER() do { s0 = ld_sc1(rs, vs, 0); s1 = ld_sc1(rs, vs + 1024, 0); cb = ld_sc1(rs, vb, 0);             \
    _Pragma("unroll") for (int k = 0; k < 10; ++k) cf[k] = ld_sc1(rs, vf + k * 12288, 0); } while (0)
  H2_GATHER();
  BAR();
  BAR();
  BAR();
  BAR();
  for (unsigned sp = 0; sp < SPIN_CAP; ++sp) {
    bool ok = (s0[1] == tag) & (s0[3] == tag) & (s1[1] == tag) & (s1[3] == tag) & (cb[1] == tag) & (cb[3] == tag);
#pragma unroll
    for (int k = 0; k < 10; ++k) ok &= (cf[k][1] == tag) & (cf[k][3] == tag);
    if (__all(ok)) break;
    __builtin_amdgcn_s_sleep(2);
    MEMBAR();
    H2_GATHER();
  }
#undef H2_GATHER
  *(LAS u32x2*)(lds + M_STAT + lane * 8) = (u32x2){s0[0], s0[2]};
  *(LAS u32x2*)(lds + M_STAT + 512 + lane * 8) = (u32x2){s1[0], s1[2]};
  if (lane < nch) {
    const int k = lane >> 4, w = lane & 15;
    *(LAS u32x2*)(lds + M_CS + ((2 * k + ((w >> 1) & 1)) * 16 + 4 * (w >> 2) + 2 * (w & 1)) * 4) = (u32x2){cb[0], cb[2]};
  }
  if (2 * lane < ncol) {
    float c0 = 0.f, c1 = 0.f;
#pragma unroll
    for (int k = 0; k < 10; ++k) { c0 += __uint_as_float(cf[k][0]); c1 += __uint_as_float(cf[k][2]); }
    *(LAS f32x2*)(lds + M_CS + (96 + 2 * lane) * 4) = (f32x2){c0, c1};
  }
  if (lane < 8) *(LAS unsigned*)(lds + M_SYNC + lane * 4) = (lane == 5) ? tag : 0u;
  WAIT_L(0);
  BAR();
  BAR();
  const int vfl = (role ? W_FLAGH : W_FLAGA) + (G.c * 16 + (lane & 15)) * 8;
  lds_spin(lds + M_SYNC + (role ? 8 : 4), 4u);
  for (unsigned sp = 0; sp < SPIN_CAP; ++sp) {
    const u32x2 f = __builtin_amdgcn_raw_buffer_load_b64(rs, vfl, 0, 16);
    if (__all((f[0] == tag) & (f[1] == ~tag))) break;
    __builtin_amdgcn_s_sleep(2);
    MEMBAR();
  }
  MEMBAR();
  if (lane == 0) *(volatile LAS unsigned*)(lds + M_SYNC + 16) = 1u;
}
__device__ __forceinline__ void service_h3(const Params& P, const Geo& G, lptr lds, rsrc_t rs, int lane) {
  const int r8 = lane >> 3, c8 = lane & 7;
  const unsigned tag = read_tag(rs);
  const int nch = G.type ? 32 : 48, chb = G.type ? 384 + 32 * G.a : 48 * G.a;
  const int jb = G.u + 128 * (r8 >> 1);
  const bool bok = (r8 < 6) && (jb < 320);
  const int jc = bok ? jb : 0;
  const int chs = (jc < 192) ? jc + 192 * G.par : 384 + (jc - 192) + 128 * G.par;
  u32x4 v[10], f[8];
  {
    const GAS char* src = (const GAS char*)((r8 & 1) ? P.Bim : P.Bre) + (size_t)chs * 1280 + c8 * 16;
#pragma unroll
    for (int q = 0; q < 10; ++q) v[q] = *(const GAS u32x4*)(src + q * 128);
  }
  const int uu = G.u < 120 ? G.u : 0;
  const int cgi = uu % 12, kr = uu / 12;
  const int cg = (cgi < 4) ? cgi + 4 * G.par : 8 + (cgi - 4) + 8 * G.par;
  {
    const GAS char* src = (const GAS char*)P.w1 + (size_t)(64 * kr + r8) * 3072 + cg * 128 + c8 * 16;
#pragma unroll
    for (int q = 0; q < 8; ++q) f[q] = *(const GAS u32x4*)(src + q * 8 * 3072);
  }
  const int chp = chb + (lane < nch ? lane : 0);
  const unsigned short wl = *(const GAS unsigned short*)((const GAS char*)P.logdt + chp * 2);
  const unsigned short wr = *(const GAS unsigned short*)((const GAS char*)P.lre + chp * 2);
  const unsigned short wi = *(const GAS unsigned short*)((const GAS char*)P.lim + chp * 2);
  BAR();
  {
    float s = 0.f;
#pragma unroll
    for (int q = 0; q < 10; ++q) s += sum8(v[q]);
    s += __shfl_xor(s, 1); s += __shfl_xor(s, 2); s += __shfl_xor(s, 4);
    const float si = __shfl_xor(s, 8);
    if (bok && (lane & 15) == 0) {
      u32x4 o = {__float_as_uint(s), tag, __float_as_uint(si), tag};
      st_sc1(o, rs, W_CSB + chs * 16, 0);
    }
    float cs[8];
#pragma unroll
    for (int e = 0; e < 4; ++e) {
      float a = 0.f, b = 0.f;
#pragma unroll
      for (int q = 0; q < 8; ++q) { a += lo_f(f[q][e]); b += hi_f(f[q][e]); }
      cs[2 * e] = a; cs[2 * e + 1] = b;
    }
#pragma unroll
    for (int e = 0; e < 8; ++e) { cs[e] += __shfl_xor(cs[e], 8); cs[e] += __shfl_xor(cs[e], 16); cs[e] += __shfl_xor(cs[e], 32); }
    const int rs4 = r8 & 3;
    const float o0 = rs4 == 0 ? cs[0] : rs4 == 1 ? cs[2] : rs4 == 2 ? cs[4] : cs[6];
    const float o1 = rs4 == 0 ? cs[1] : rs4 == 1 ? cs[3] : rs4 == 2 ? cs[5] : cs[7];
    if (G.u < 120 && r8 < 4) {
      u32x4 o = {__float_as_uint(o0), tag, __float_as_uint(o1), tag};
      st_sc1(o, rs, W_CSF + kr * 12288 + (64 * cg + 8 * c8 + 2 * rs4) * 8, 0);
    }
  }
  BAR();
  f32x2 L, CF, L4, L8, L16, L32, L64, L128;
  {
    const float dt = bfr(expf(bf2f(wl)));
    const float ar = bf2f(wr), ai = bf2f(wi);
    const float zr = dt * ar, zi = dt * ai;
    const float ex = expf(zr);
    L.x = ex * cosf(zi); L.y = ex * sinf(zi);
    const float nr = L.x - 1.f, ni = L.y, dd = ar * ar + ai * ai;
    CF.x = (nr * ar + ni * ai) / dd; CF.y = (ni * ar - nr * ai) / dd;
    const f32x2 L2 = cmul(L, L);
    L4 = cmul(L2, L2); L8 = cmul(L4, L4); L16 = cmul(L8, L8); L32 = cmul(L16, L16); L64 = cmul(L32, L32); L128 = cmul(L64, L64);
  }
  BAR();
  BAR();
  BAR();
  BAR();
  BAR();
  BAR();
  BAR();
  if (lane < nch) {
    lptr tb = lds + M_TAB + lane * 8;
    *(LAS f32x2*)(tb) = L; *(LAS f32x2*)(tb + 384) = L4; *(LAS f32x2*)(tb + 768) = L8;
    *(LAS f32x2*)(tb + 1152) = L16; *(LAS f32x2*)(tb + 1536) = L32; *(LAS f32x2*)(tb + 1920) = CF;
    f32x2 pw = {1.f, 0.f};
#pragma unroll
    for (int k = 0; k < 16; ++k) { *(LAS f32x2*)(lds + M_P4 + k * 400 + lane * 8) = pw; pw = cmul(pw, L4); }
  }
  WAIT_L(0);
  BAR();
  BAR();
  lds_spin(lds + M_SYNC, 4u);
  const int cl = lane < nch ? lane : 0;
  const f32x2 e0 = *(const LAS f32x2*)(lds + M_ENDW + cl * 8), e1 = *(const LAS f32x2*)(lds + M_ENDW + (48 + cl) * 8);
  const int ve = W_EGR + (chb + cl) * 16;
  if (G.c < 15 && lane < nch) {
    const f32x2 ec = cfma(L64, e0, e1);
    u32x4 o = {__float_as_uint(ec.x), tag, __float_as_uint(ec.y), tag};
    st_sc1(o, rs, ve + G.c * 10240, 0);
  }
  f32x2 S = {0.f, 0.f};
  if (G.c > 0) {
    u32x4 ev[15];
    for (unsigned sp = 0; sp < SPIN_CAP; ++sp) {
      bool ok = true;
#pragma unroll
      for (int j = 0; j < 15; ++j) ev[j] = ld_sc1(rs, ve + (j < G.c ? j : 0) * 10240, 0);
#pragma unroll
      for (int j = 0; j < 15; ++j) ok &= (ev[j][1] == tag) & (ev[j][3] == tag);
      if (__all(ok)) break;
      __builtin_amdgcn_s_sleep(1);
      MEMBAR();
    }
#pragma unroll
    for (int j = 0; j < 15; ++j)
      if (j < G.c) S = cfma(L128, S, (f32x2){__uint_as_float(ev[j][0]), __uint_as_float(ev[j][2])});
  }
  if (lane < nch) {
    *(LAS f32x2*)(lds + M_SIN + lane * 8) = S;
    *(LAS f32x2*)(lds + M_SIN + (48 + lane) * 8) = cfma(L64, S, e0);
  }
  WAIT_L(0);
  if (lane == 0) *(volatile LAS unsigned*)(lds + M_SYNC + 12) = 1u;
}

__global__ __launch_bounds__(512, 2) void fused_kernel(const Params P) {
  __shared__ __attribute__((aligned(1024))) char lds_[163840];
  lptr lds = (lptr)lds_;
  const int tid = threadIdx.x, wid = __builtin_amdgcn_readfirstlane(tid >> 6), lane = tid & 63;
  const int b = blockIdx.x, xcd = b & 7, r = b >> 3;
  Geo G;
  G.c = 4 * (xcd >> 1) + (r & 3); G.jj = r >> 2; G.type = G.jj & 1; G.par = xcd & 1;
  G.a = 4 * G.par + (G.jj >> 1); G.pid = 8 * G.par + G.jj; G.u = (xcd >> 1) * 32 + r;
  const int role = G.jj >= 5;
  const int yt = G.jj - 5;
  const int t2 = role ? 20 * G.par + (yt == 0 ? 0 : yt == 1 ? 7 : 13) : 5 * G.par + G.jj;
  const int q0 = role ? 3 * yt : (G.jj * 12) / 5;
  const rsrc_t rs = __builtin_amdgcn_make_buffer_rsrc((void*)P.ws, 0, W_END, 0x00020000);
  TS(0);
  if (wid < 4) {
    const int wn = wid & 1;
    if (G.type == 0 && wn == 0) p1_mfma<2, 0>(P, G, lds, rs, wid, lane, 0, 28672, 0, 48 + 3 * G.a, 96);
    else {
      const int pair0 = G.type ? wn : 2;
      const int foff = G.type ? 24576 + wn * 8192 : 28672;
      const int fstep0 = G.type ? 16 + 4 * G.a + 2 * wn : 2 * G.a;
      const int hstep0 = G.type ? 72 + 2 * G.a + wn : 48 + 3 * G.a + 2;
      const int csf0 = G.type ? 96 + 64 * wn : 96;
      p1_mfma<1, 2>(P, G, lds, rs, wid, lane, pair0, foff, fstep0, hstep0, csf0);
    }
    if (role) p2_y(P, G, lds, rs, wid, lane, t2, yt == 2, q0);
    else p2_o(P, G, lds, rs, wid, lane, t2, q0);
  } else if (wid < 6) {
    const int H = wid - 4;
    if (G.type == 0) { if (role) loader<0, 1>(P, G, lds, rs, H, lane, t2, q0); else loader<0, 0>(P, G, lds, rs, H, lane, t2, q0); }
    else { if (role) loader<1, 1>(P, G, lds, rs, H, lane, t2, q0); else loader<1, 0>(P, G, lds, rs, H, lane, t2, q0); }
    if (b == 0 && H == 0) {
      const unsigned tag = lds_ld(lds + M_SYNC + 20);
      for (unsigned sp = 0; sp < SPIN_CAP; ++sp) {
        const u32x4 f0 = ld_sc1(rs, W_FLAGH + lane * 32, 0), f1 = ld_sc1(rs, W_FLAGH + lane * 32 + 16, 0);
        const bool ok = (f0[0] == tag) & (f0[1] == ~tag) & (f0[2] == tag) & (f0[3] == ~tag) & (f1[0] == tag) & (f1[1] == ~tag) & (f1[2] == tag) & (f1[3] == ~tag);
        if (__all(ok)) break;
        __builtin_amdgcn_s_sleep(2);
        MEMBAR();
      }
      if (lane == 0) __builtin_amdgcn_raw_buffer_store_b32(tag, rs, W_EPOCH, 0, 16);
    }
  } else if (wid == 6) {
    service_h2(P, G, lds, rs, lane, role);
  } else {
    service_h3(P, G, lds, rs, lane);
  }
  TS(9);
}

extern "C" void kernel_launch(void* const* d_in, const int* in_sizes, int n_in, void* d_out, int out_size, void* d_ws,
                              size_t ws_size, hipStream_t stream) {
  Params P;
  P.x = (const char*)d_in[0]; P.lre = (const char*)d_in[1]; P.lim = (const char*)d_in[2];
  P.Bre = (const char*)d_in[3]; P.Bim = (const char*)d_in[4]; P.Cre = (const char*)d_in[5]; P.Cim = (const char*)d_in[6];
  P.logdt = (const char*)d_in[7]; P.dskip = (const char*)d_in[8]; P.w1 = (const char*)d_in[10]; P.w2 = (const char*)d_in[11];
  P.ws = (char*)d_ws; P.out = (float*)d_out;
  fused_kernel<<<256, 512, 0, stream>>>(P);
}
```

```cpp
#include <hip/hip_runtime.h>
#include <hip/hip_bf16.h>
#include <stdint.h>

typedef __attribute__((ext_vector_type(8))) short bf16x8;
typedef __attribute__((ext_vector_type(4))) short s16x4;
typedef __attribute__((ext_vector_type(4))) float f32x4;
typedef __attribute__((ext_vector_type(2))) float f32x2;
typedef __attribute__((ext_vector_type(4))) unsigned u32x4;
typedef __attribute__((ext_vector_type(2))) unsigned u32x2;
typedef __attribute__((ext_vector_type(2))) __bf16 bf2_t;
#define GAS __attribute__((address_space(1)))
#define LAS __attribute__((address_space(3)))
typedef LAS char* lptr;

#define WAIT_V(n) asm volatile("s_waitcnt vmcnt(%0)" ::"n"(n) : "memory")
#define WAIT_L(n) asm volatile("s_waitcnt lgkmcnt(%0)" ::"n"(n) : "memory")
#define SCHED() __builtin_amdgcn_sched_barrier(0)
#define MEMBAR() asm volatile("" ::: "memory")
#define BAR() do { MEMBAR(); __builtin_amdgcn_s_barrier(); MEMBAR(); } while (0)

constexpr int W_EPOCH = 0;
constexpr int W_FLAGA = 4096;
constexpr int W_FLAGH = 12288;
constexpr int W_STAT  = 20480;
constexpr int W_CSB   = 53248;
constexpr int W_CSF   = 65536;
constexpr int W_EGR   = 196608;
constexpr int W_AF    = 393216;
constexpr int W_GB    = W_AF + 16 * 8 * 88 * 1024;
constexpr int W_END   = W_GB + 16 * 4 * 40 * 2048;
constexpr int W_DBG   = W_END;
constexpr int MISC   = 155648;
constexpr int M_FULL = MISC;
constexpr int M_PROG = MISC + 16;
constexpr int M_SVCA = MISC + 32, M_SVCB = MISC + 36, M_CNTE = MISC + 40, M_CARRY = MISC + 44, M_GO = MISC + 48, M_TAG = MISC + 52;
constexpr int M_STAT = MISC + 256;
constexpr int M_CS   = MISC + 1280;
constexpr int M_TAB  = MISC + 2304;
constexpr int M_ENDW = MISC + 6144;
constexpr int M_SIN  = MISC + 6912;
constexpr int SLOT2  = 20480;
constexpr unsigned SPIN_CAP = 1u << 16;

__device__ __forceinline__ float bf2f(unsigned short v) { return __uint_as_float((unsigned)v << 16); }
__device__ __forceinline__ float bfr(float a) { return (float)(__bf16)a; }
__device__ __forceinline__ unsigned pack2(float a, float b) {
  bf2_t v = {(__bf16)a, (__bf16)b};
  return __builtin_bit_cast(unsigned, v);
}
__device__ __forceinline__ float lo_f(unsigned u) { return __uint_as_float(u << 16); }
__device__ __forceinline__ float hi_f(unsigned u) { return __uint_as_float(u & 0xFFFF0000u); }
__device__ __forceinline__ f32x2 cmul(f32x2 a, f32x2 b) {
  f32x2 r; r.x = a.x * b.x - a.y * b.y; r.y = a.x * b.y + a.y * b.x; return r;
}
__device__ __forceinline__ f32x2 cfma(f32x2 a, f32x2 b, f32x2 c) {
  f32x2 r; r.x = fmaf(a.x, b.x, fmaf(-a.y, b.y, c.x)); r.y = fmaf(a.x, b.y, fmaf(a.y, b.x, c.y)); return r;
}
struct C4 { f32x4 re, im; };
__device__ __forceinline__ f32x4 fma4(f32x4 a, f32x4 b, f32x4 c) { return __builtin_elementwise_fma(a, b, c); }
__device__ __forceinline__ C4 cfma4(const C4& a, const C4& b, const C4& c) {
  C4 r; r.re = fma4(a.re, b.re, fma4(-a.im, b.im, c.re)); r.im = fma4(a.re, b.im, fma4(a.im, b.re, c.im)); return r;
}
__device__ __forceinline__ C4 cmul4(const C4& a, const C4& b) {
  C4 r; r.re = fma4(a.re, b.re, -(a.im * b.im)); r.im = fma4(a.re, b.im, a.im * b.re); return r;
}
template <int N> __device__ __forceinline__ float row_shr(float v) {
  return __builtin_bit_cast(float, __builtin_amdgcn_update_dpp(0, __builtin_bit_cast(int, v), 0x110 + N, 0xf, 0xf, true));
}
template <int N> __device__ __forceinline__ C4 row_shr4(const C4& v) {
  C4 r;
#pragma unroll
  for (int e = 0; e < 4; ++e) { r.re[e] = row_shr<N>(v.re[e]); r.im[e] = row_shr<N>(v.im[e]); }
  return r;
}
__device__ __forceinline__ u32x2 silu_pack4(f32x4 v) {
  const unsigned p0 = pack2(v[0], v[1]), p1 = pack2(v[2], v[3]);
  const f32x4 x = {lo_f(p0), hi_f(p0), lo_f(p1), hi_f(p1)};
  const f32x4 t = x * (-1.44269504088896341f);
  f32x4 d;
#pragma unroll
  for (int e = 0; e < 4; ++e) d[e] = __builtin_amdgcn_exp2f(t[e]);
  d = d + 1.f;
#pragma unroll
  for (int e = 0; e < 4; ++e) d[e] = __builtin_amdgcn_rcpf(d[e]);
  const f32x4 s = x * d;
  return (u32x2){pack2(s[0], s[1]), pack2(s[2], s[3])};
}
__device__ __forceinline__ float sum8(u32x4 v) {
  return ((lo_f(v[0]) + hi_f(v[0])) + (lo_f(v[1]) + hi_f(v[1]))) + ((lo_f(v[2]) + hi_f(v[2])) + (lo_f(v[3]) + hi_f(v[3])));
}
typedef __amdgpu_buffer_rsrc_t rsrc_t;
__device__ __forceinline__ rsrc_t mk_rsrc(const void* p, int bytes) { return __builtin_amdgcn_make_buffer_rsrc((void*)p, 0, bytes, 0x00020000); }
__device__ __forceinline__ void dma(rsrc_t r, int voff, int soff, unsigned l) {
  __builtin_amdgcn_raw_ptr_buffer_load_lds(r, (LAS void*)(size_t)l, 16, voff, soff, 0, 0);
}
__device__ __forceinline__ bf16x8 lds_b128(unsigned a) { return *(const LAS bf16x8*)(size_t)a; }
__device__ __forceinline__ f32x4 lds_f4(unsigned a) { return *(const LAS f32x4*)(size_t)a; }
__device__ __forceinline__ s16x4 lds_tr(unsigned a) { return __builtin_amdgcn_ds_read_tr16_b64_v4i16((LAS s16x4*)(size_t)a); }
__device__ __forceinline__ bf16x8 cat8(s16x4 a, s16x4 b) { return __builtin_shufflevector(a, b, 0, 1, 2, 3, 4, 5, 6, 7); }
__device__ __forceinline__ unsigned opaque(unsigned v) { asm volatile("" : "+v"(v)); return v; }
__device__ __forceinline__ u32x4 ld_sc1(rsrc_t r, int voff, int soff) { return __builtin_amdgcn_raw_buffer_load_b128(r, voff, soff, 16); }
__device__ __forceinline__ void st_sc1(u32x4 v, rsrc_t r, int voff, int soff) { __builtin_amdgcn_raw_buffer_store_b128(v, r, voff + soff, 0, 16); }
__device__ __forceinline__ unsigned lds_ld(unsigned a) { return *(volatile LAS unsigned*)(size_t)a; }
__device__ __forceinline__ void lds_st1(unsigned a, unsigned v) {
  asm volatile("s_mov_b64 exec, 1\n\tds_write_b32 %0, %1\n\ts_mov_b64 exec, -1" ::"v"(a), "v"(v) : "memory");
}
__device__ __forceinline__ void spin_ge(unsigned a, unsigned need) {
#pragma clang loop unroll(disable)
  for (unsigned s = 0; s < (1u << 22); ++s) {
    if (__builtin_amdgcn_readfirstlane(lds_ld(a)) >= need) break;
    __builtin_amdgcn_s_sleep(1);
  }
  MEMBAR();
}
__device__ __forceinline__ void spin_prog(unsigned need) {
#pragma clang loop unroll(disable)
  for (unsigned s = 0; s < (1u << 22); ++s) {
    const u32x4 p = *(volatile LAS u32x4*)(size_t)M_PROG;
    const unsigned a = p[0] < p[1] ? p[0] : p[1], b = p[2] < p[3] ? p[2] : p[3];
    if (__builtin_amdgcn_readfirstlane(a < b ? a : b) >= need) break;
    __builtin_amdgcn_s_sleep(1);
  }
  MEMBAR();
}
__device__ __forceinline__ void lds_inc(unsigned a) {
  if ((threadIdx.x & 63) == 0) __hip_atomic_fetch_add((LAS unsigned*)(size_t)a, 1u, __ATOMIC_RELAXED, __HIP_MEMORY_SCOPE_WORKGROUP);
}
#define TS(k) do {} while (0)
#define TC(k) do {} while (0)

struct Params {
  const char *x, *lre, *lim, *Bre, *Bim, *Cre, *Cim, *logdt, *dskip, *w1, *w2;
  char* ws;
  float* out;
};
struct Geo {
  int c, type, a, pid, par, u, jj;
};

template <int NB, int NF, bool RD, bool CHK, bool PRE>
__device__ __forceinline__ void p1_half(f32x4 (&acc)[4][2 * NB + 2 * NF], const bf16x8 (&ca)[4], const bf16x8 (&cw)[2 * NB + 2 * NF],
                                        bf16x8 (&na)[4], bf16x8 (&nw)[2 * NB + 2 * NF], unsigned vA, unsigned vB, const unsigned (&aF)[4],
                                        unsigned fo, unsigned& fl, unsigned flchk, unsigned need, unsigned flpre, unsigned vprog, unsigned prog) {
  constexpr int NT = 2 * NB + 2 * NF;
#pragma unroll
  for (int j = 0; j < 4 * NT; ++j) {
    acc[j & 3][j >> 2] = __builtin_amdgcn_mfma_f32_16x16x32_bf16(cw[j >> 2], ca[j & 3], acc[j & 3][j >> 2], 0, 0, 0);
    SCHED();
    if (j == 1) {
      WAIT_L(0);
      lds_st1(vprog, prog);
      if (CHK) { if (__builtin_amdgcn_readfirstlane(fl) < need) spin_ge(flchk, need); }
      SCHED();
    }
    if (RD) {
      if (j >= 2 && j < 6) na[j - 2] = lds_b128(vA + (j - 2) * 2048);
      else if (j >= 6 && j < 6 + 2 * NB) nw[j - 6] = lds_b128(vB + (j - 6) * 2048);
      else if (j >= 6 + 2 * NB && j < 6 + NT) {
        const unsigned q = aF[j - 6 - 2 * NB] + fo;
        nw[j - 6] = cat8(lds_tr(q), lds_tr(q + 512));
      }
    }
    if (PRE && j == 6 + NT) fl = lds_ld(flpre);
    SCHED();
  }
}

template <int NB, int NF>
__device__ __forceinline__ void p1_mfma(const Params& P, const Geo& G, rsrc_t rs, int wid, int lane, int pair0, int offF, int strB, int strF,
                                        int fstep0, int hstep0, int csf0) {
  constexpr int NT = 2 * NB + 2 * NF;
  const int g = lane >> 4, i = lane & 15, wm = wid >> 1;
  f32x4 acc[4][NT];
#pragma unroll
  for (int m = 0; m < 4; ++m)
#pragma unroll
    for (int n = 0; n < NT; ++n) acc[m][n] = (f32x4){0.f, 0.f, 0.f, 0.f};
  const int sw = (g ^ (i & 7)) * 16;
  const unsigned aA = wm * 8192 + i * 128 + sw, aB = 65536 + pair0 * 4096 + i * 128 + sw;
  const int sg = ((i >> 3) & 1) | ((g & 1) << 1);
  unsigned aF[4];
#pragma unroll
  for (int n = 0; n < 4; ++n) aF[n] = offF + (8 * g + (i >> 2)) * 128 + ((n ^ sg) * 32) + 8 * (i & 3);
  const unsigned vprog = M_PROG + wid * 4;
  bf16x8 a0[4], w0[NT], a1[4], w1[NT];
  BAR();
  TS(1);
  spin_ge(M_FULL, 1u);
  TS(10);
#pragma unroll
  for (int m = 0; m < 4; ++m) a0[m] = lds_b128(aA + m * 2048);
#pragma unroll
  for (int n = 0; n < 2 * NB; ++n) w0[n] = lds_b128(aB + n * 2048);
#pragma unroll
  for (int n = 0; n < 2 * NF; ++n) w0[2 * NB + n] = cat8(lds_tr(aF[n]), lds_tr(aF[n] + 512));
  unsigned fl = 0;
  int fs = 1;
#define NEXT_FS() do { fs = (fs == 6) ? 0 : fs + 1; } while (0)
#pragma unroll 1
  for (int s = 0; s < 9; ++s) {
    const unsigned flw = M_FULL + ((s + 1) & 1) * 4;
    p1_half<NB, NF, true, false, true>(acc, a0, w0, a1, w1, (aA + (s & 3) * 16384) ^ 64, (aB + (s & 3) * strB) ^ 64, aF, fs * strF, fl, 0, 0, flw, vprog, 2 * s + 1);
    NEXT_FS();
    p1_half<NB, NF, true, true, false>(acc, a1, w1, a0, w0, aA + ((s + 1) & 3) * 16384, aB + ((s + 1) & 3) * strB, aF, fs * strF, fl, flw, s + 2, 0, vprog, 2 * s + 2);
    NEXT_FS();
  }
  p1_half<NB, NF, true, false, false>(acc, a0, w0, a1, w1, (aA + 16384) ^ 64, (aB + strB) ^ 64, aF, 5 * strF, fl, 0, 0, 0, vprog, 19);
  p1_half<NB, NF, false, false, false>(acc, a1, w1, a0, w0, 0, 0, aF, 0, fl, 0, 0, 0, vprog, 20);
#undef NEXT_FS
  TS(2);
  spin_ge(M_SVCA, 1u);
  spin_ge(M_SVCB, 1u);
  TS(11);
  float mb[4], rd[4];
  {
    const f32x4 s0 = lds_f4(M_STAT + (64 * wm + 4 * i) * 8), s1 = lds_f4(M_STAT + (64 * wm + 4 * i) * 8 + 16);
    mb[0] = s0[0]; mb[1] = s0[2]; mb[2] = s1[0]; mb[3] = s1[2];
    rd[0] = s0[1]; rd[1] = s0[3]; rd[2] = s1[1]; rd[3] = s1[3];
  }
  const unsigned tag = lds_ld(M_TAG);
#define TABLE(k, pos) (C4){lds_f4(M_TAB + (k) * 384 + (pos) * 4), lds_f4(M_TAB + (k) * 384 + 192 + (pos) * 4)}
  C4 Ep[NB];
#pragma unroll
  for (int p = 0; p < NB; ++p) {
    const int pos = 16 * (pair0 + p) + 4 * g;
    const f32x4 csr = lds_f4(M_CS + pos * 4), csi = lds_f4(M_CS + 192 + pos * 4);
    const C4 L1 = TABLE(0, pos);
    C4 h;
#pragma unroll
    for (int m = 0; m < 4; ++m) {
      C4 bu;
      bu.re = fma4(csr, (f32x4)(-mb[m]), acc[m][2 * p]) * rd[m];
      bu.im = fma4(csi, (f32x4)(-mb[m]), acc[m][2 * p + 1]) * rd[m];
      h = (m == 0) ? bu : cfma4(L1, h, bu);
      acc[m][2 * p] = h.re; acc[m][2 * p + 1] = h.im;
    }
    C4 e = h;
    e = cfma4(TABLE(1, pos), row_shr4<1>(e), e);
    e = cfma4(TABLE(2, pos), row_shr4<2>(e), e);
    e = cfma4(TABLE(3, pos), row_shr4<4>(e), e);
    e = cfma4(TABLE(4, pos), row_shr4<8>(e), e);
    if (i == 15) {
      *(LAS f32x4*)(size_t)(M_ENDW + wm * 384 + pos * 4) = e.re;
      *(LAS f32x4*)(size_t)(M_ENDW + wm * 384 + 192 + pos * 4) = e.im;
    }
    Ep[p] = row_shr4<1>(e);
  }
  WAIT_L(0);
  lds_inc(M_CNTE);
  TS(12);
  const int afv = W_AF + (G.c * 8 + 4 * wm) * 90112 + lane * 16;
  const u32x2 flg = {tag, ~tag};
  if (NF > 0) {
#pragma unroll
    for (int f = 0; f < NF; ++f) {
      const f32x4 cs0 = lds_f4(M_CS + (csf0 + 32 * f + 4 * g) * 4), cs1 = lds_f4(M_CS + (csf0 + 32 * f + 16 + 4 * g) * 4);
#pragma unroll
      for (int m = 0; m < 4; ++m) {
        const u32x2 o0 = silu_pack4(fma4(cs0, (f32x4)(-mb[m]), acc[m][2 * NB + 2 * f]) * rd[m]);
        const u32x2 o1 = silu_pack4(fma4(cs1, (f32x4)(-mb[m]), acc[m][2 * NB + 2 * f + 1]) * rd[m]);
        st_sc1((u32x4){o0[0], o0[1], o1[0], o1[1]}, rs, afv + m * 90112 + f * 1024, fstep0 * 1024);
      }
    }
  }
  TS(13);
  C4 Q[NB][4];
#pragma unroll
  for (int p = 0; p < NB; ++p) {
    const int pos = 16 * (pair0 + p) + 4 * g;
    const C4 CF = TABLE(5, pos);
    C4 pw;
#pragma unroll
    for (int k = 0; k < 4; ++k) {
      C4 t = TABLE(1 + k, pos);
      const bool on = (i >> k) & 1;
#pragma unroll
      for (int e = 0; e < 4; ++e) { t.re[e] = on ? t.re[e] : 1.f; t.im[e] = on ? t.im[e] : 0.f; }
      pw = (k == 0) ? t : cmul4(pw, t);
    }
#pragma unroll
    for (int m = 0; m < 4; ++m) {
      const C4 CL = TABLE(6 + m, pos);
      const C4 l = cfma4(CL, Ep[p], cmul4(CF, (C4){acc[m][2 * p], acc[m][2 * p + 1]}));
      acc[m][2 * p] = l.re; acc[m][2 * p + 1] = l.im;
      Q[p][m] = cmul4(CL, pw);
    }
    if (p == 0) {
      TS(14);
      WAIT_V(0);
      if (lane == 0) __builtin_amdgcn_raw_buffer_store_b64(flg, rs, W_FLAGA + ((G.c * 16 + G.pid) * 4 + wid) * 8, 0, 16);
      TS(3);
    }
  }
#undef TABLE
  spin_ge(M_CARRY, 1u);
  TS(4);
#pragma unroll
  for (int p = 0; p < NB; ++p) {
    const int pos = 16 * (pair0 + p) + 4 * g;
    const C4 S = {lds_f4(M_SIN + wm * 384 + pos * 4), lds_f4(M_SIN + wm * 384 + 192 + pos * 4)};
#pragma unroll
    for (int m = 0; m < 4; ++m) {
      const C4 h = cfma4(Q[p][m], S, (C4){acc[m][2 * p], acc[m][2 * p + 1]});
      const u32x2 x0 = __builtin_amdgcn_permlane32_swap(pack2(h.re[0], h.re[1]), pack2(-h.im[0], -h.im[1]), false, false);
      const u32x2 x1 = __builtin_amdgcn_permlane32_swap(pack2(h.re[2], h.re[3]), pack2(-h.im[2], -h.im[3]), false, false);
      st_sc1((u32x4){x0[0], x1[0], x0[1], x1[1]}, rs, afv + m * 90112 + p * 1024, hstep0 * 1024);
    }
  }
  TS(15);
  WAIT_V(0);
  if (lane == 0) __builtin_amdgcn_raw_buffer_store_b64(flg, rs, W_FLAGH + ((G.c * 16 + G.pid) * 4 + wid) * 8, 0, 16);
  TS(5);
}

template <int ROLE>
__device__ __forceinline__ void p2_mfma(const Params& P, const Geo& G, rsrc_t rs, int wid, int lane, int ct, int q0) {
  constexpr int NS = ROLE ? 40 : 48, NQ = NS / 4, DP = ROLE ? 14 : 16;
  const int g = lane >> 4, i = lane & 15;
  const int tl = 64 * (wid >> 1) + 4 * i + 2 * (wid & 1);
  const GAS char* xrow = (const GAS char*)P.x + (size_t)(128 * G.c + tl) * 1280 + (80 * ct + 4 * g) * 2;
  f32x4 acc[2][5];
  if (ROLE) {
    const f32x4 st = lds_f4(M_STAT + tl * 8);
#pragma unroll
    for (int n = 0; n < 5; ++n) {
      const u32x2 dv = *(const GAS u32x2*)((const GAS char*)P.dskip + (80 * ct + 16 * n + 4 * g) * 2);
      const f32x4 df = {lo_f(dv[0]), hi_f(dv[0]), lo_f(dv[1]), hi_f(dv[1])};
#pragma unroll
      for (int m = 0; m < 2; ++m) {
        const u32x2 xv = *(const GAS u32x2*)(xrow + m * 1280 + n * 32);
        const f32x4 xf = {lo_f(xv[0]), hi_f(xv[0]), lo_f(xv[1]), hi_f(xv[1])};
#pragma unroll
        for (int e = 0; e < 4; ++e) acc[m][n][e] = 0.5f * bfr(bfr(bfr(xf[e] - st[2 * m]) * st[2 * m + 1]) * df[e]);
        asm volatile("" : "+v"(acc[m][n]));
      }
    }
  } else {
#pragma unroll
    for (int m = 0; m < 2; ++m)
#pragma unroll
      for (int n = 0; n < 5; ++n) acc[m][n] = (f32x4){0.f, 0.f, 0.f, 0.f};
  }
  unsigned bW[4];
  if (ROLE) {
#pragma unroll
    for (int k = 0; k < 4; ++k) bW[k] = (g >> 1) * 10240 + i * 128 + (((2 * k + (g & 1)) ^ (i & 7)) * 16);
  } else bW[0] = (4 * g + (i >> 2)) * 160 + 8 * (i & 3);
  const int v0 = W_AF + (G.c * 8 + 2 * wid) * 90112 + (ROLE ? 48 * 1024 : 0) + lane * 16, v1 = v0 + 90112;
  const int gv = W_GB + ((G.c * 4 + wid) * 40 + 5 * ct) * 2048 + lane * 16;
  const unsigned vprog = M_PROG + wid * 4;
  const unsigned tag = lds_ld(M_TAG);
  u32x4 ra[DP][2], gq[5][2];
  u32x2 xq[5][2];
  bf16x8 wf[2][5];
  unsigned fl = 0, vb = 0;
#define P2_SOFF(s) ((((((s) >> 2) + q0) >= NQ) ? ((s) >> 2) + q0 - NQ : ((s) >> 2) + q0) * 4096)
#define P2_BASE(s) opaque(ROLE ? bW[(s) & 3] + (((s) >> 2) % 7) * SLOT2 : bW[0] + (((s) >> 2) % 7) * SLOT2)
#define P2_READ(s, n) (ROLE ? lds_b128(vb + (n) * 2048) : cat8(lds_tr(vb + ((s) & 3) * 5120 + (n) * 32), lds_tr(vb + ((s) & 3) * 5120 + 2560 + (n) * 32)))
  spin_ge(M_FULL, 11u);
  vb = P2_BASE(0);
#pragma unroll
  for (int n = 0; n < 5; ++n) wf[0][n] = P2_READ(0, n);
  spin_ge(M_GO, 1u);
  __builtin_amdgcn_s_waitcnt(0x0F70);
  TS(6);
#pragma unroll
  for (int s = 0; s < DP - 1; ++s) { ra[s][0] = ld_sc1(rs, v0 + (s & 3) * 1024, P2_SOFF(s)); ra[s][1] = ld_sc1(rs, v1 + (s & 3) * 1024, P2_SOFF(s)); }
  SCHED();
#pragma unroll
  for (int s = 0; s < NS; ++s) {
    const int r0 = ((s & 3) == 3) ? 2 : 1;
#pragma unroll
    for (int j = 0; j < 10; ++j) {
      acc[j & 1][j >> 1] = __builtin_amdgcn_mfma_f32_16x16x32_bf16(wf[s & 1][j >> 1], __builtin_bit_cast(bf16x8, ra[s % DP][j & 1]), acc[j & 1][j >> 1], 0, 0, 0);
      SCHED();
      if (j == 0 && s + 1 < NS && (ROLE || (s & 3) == 3)) vb = P2_BASE(s + 1);
      if ((s & 3) == 3 && j == 1) {
        WAIT_L(0);
        lds_st1(vprog, 21 + (s >> 2));
        TS(40 + (s >> 2));
        if (s + 1 < NS) { if (__builtin_amdgcn_readfirstlane(fl) < 12u + (s >> 2)) spin_ge(M_FULL + (((s >> 2) + 1) & 1) * 4, 12u + (s >> 2)); }
      }
      if (s + 1 < NS && j >= r0 && j < r0 + 5) wf[(s + 1) & 1][j - r0] = P2_READ(s + 1, j - r0);
      if ((s & 3) == 2 && j == 6 && s + 2 < NS) fl = lds_ld(M_FULL + (((s >> 2) + 1) & 1) * 4);
      if (j == 7 || j == 8) {
        const int sl = s - 1 + DP;
        if (sl < NS) ra[sl % DP][j - 7] = ld_sc1(rs, (j == 7 ? v0 : v1) + (sl & 3) * 1024, P2_SOFF(sl));
        else if (ROLE && s >= 28 && s < 33) xq[s - 28][j - 7] = *(const GAS u32x2*)(xrow + (j - 7) * 1280 + (s - 28) * 32);
        else if (ROLE && s >= 35) gq[s - 35][j - 7] = ld_sc1(rs, gv + (s - 35) * 2048 + (j - 7) * 1024, 0);
      }
      SCHED();
    }
  }
#undef P2_SOFF
#undef P2_BASE
#undef P2_READ
  TS(7);
  if (ROLE == 0) {
#pragma unroll
    for (int n = 0; n < 5; ++n)
#pragma unroll
      for (int m = 0; m < 2; ++m) {
        const u32x2 s = silu_pack4(acc[m][n]);
        st_sc1((u32x4){s[0], tag, s[1], tag}, rs, gv + n * 2048 + m * 1024, 0);
      }
  } else {
#pragma clang loop unroll(disable)
    for (unsigned sp = 0; sp < SPIN_CAP; ++sp) {
      bool ok = true;
#pragma unroll
      for (int n = 0; n < 5; ++n)
#pragma unroll
        for (int m = 0; m < 2; ++m) ok &= (gq[n][m][1] == tag) & (gq[n][m][3] == tag);
      if (__all(ok)) break;
      __builtin_amdgcn_s_sleep(2);
      MEMBAR();
#pragma unroll
      for (int n = 0; n < 5; ++n) { gq[n][0] = ld_sc1(rs, gv + n * 2048, 0); gq[n][1] = ld_sc1(rs, gv + n * 2048 + 1024, 0); }
    }
    GAS char* orow = (GAS char*)P.out + (size_t)(128 * G.c + tl) * 2560 + (80 * ct + 4 * g) * 4;
#pragma unroll
    for (int n = 0; n < 5; ++n)
#pragma unroll
      for (int m = 0; m < 2; ++m) {
        const f32x4 xf = {lo_f(xq[n][m][0]), hi_f(xq[n][m][0]), lo_f(xq[n][m][1]), hi_f(xq[n][m][1])};
        const f32x4 gf = {lo_f(gq[n][m][0]), hi_f(gq[n][m][0]), lo_f(gq[n][m][2]), hi_f(gq[n][m][2])};
        *(GAS f32x4*)(orow + m * 2560 + n * 64) = xf + (acc[m][n] * 2.f) * gf;
      }
  }
  TS(8);
}

template <int TYPE>
__device__ __forceinline__ void loader1(const Params& P, const Geo& G, int H, int lane) {
  constexpr int NPB = TYPE ? 2 : 3, NCG = TYPE ? 2 : 1;
  constexpr int STRB = TYPE ? 8192 : 12288, OFFF = TYPE ? 98304 : 114688, STRF = TYPE ? 8192 : 4096;
  constexpr int NP = 16 + 4 * NPB + 8 * NCG;
  const int r8 = lane >> 3, c8 = lane & 7;
  const int chb = TYPE ? 384 + 32 * G.a : 48 * G.a;
  const int colb = TYPE ? 512 + 128 * G.a : 64 * G.a;
  const rsrc_t rX = mk_rsrc(P.x, 2048 * 1280), rBr = mk_rsrc(P.Bre, 640 * 1280), rBi = mk_rsrc(P.Bim, 640 * 1280), rW = mk_rsrc(P.w1, 640 * 3072);
  const int vA = 4 * r8 * 1280 + ((c8 ^ r8) * 16);
  const int vB = (8 * (r8 >> 2) + (r8 & 3)) * 1280 + ((c8 ^ r8) * 16);
  int vF[2];
#pragma unroll
  for (int h = 0; h < 2; ++h) vF[h] = (8 * h + r8) * 3072 + ((((c8 >> 1) ^ (((r8 >> 1) & 1) | (h << 1))) << 1) | (c8 & 1)) * 16;
  const int sA = G.c * (128 * 1280), sB = chb * 1280, sF = colb * 2;
  auto issueF = [&](int s, int x) {
    int f = 2 * s + x; f = f >= 14 ? f - 14 : f >= 7 ? f - 7 : f;
#pragma unroll
    for (int q = 0; q < 4 * NCG; ++q)
      dma(rW, vF[q & 1], sF + (64 * s + 32 * x + 16 * ((q >> 1) & 1)) * 3072 + (q >> 2) * 128, OFFF + f * STRF + q * 1024);
  };
  auto issue = [&](int s, bool guard) {
    const int sl = s & 3;
#pragma unroll
    for (int q = 0; q < 16; ++q)
      dma(rX, vA, sA + (64 * (q >> 3) + 32 * (q & 1) + ((q >> 1) & 3)) * 1280 + 128 * s, sl * 16384 + q * 1024);
#pragma unroll
    for (int q = 0; q < 4 * NPB; ++q) {
      if (guard && q == 4) WAIT_V(40);
      dma(((q >> 1) & 1) ? rBi : rBr, vB, sB + (16 * (q >> 2) + 4 * (q & 1)) * 1280 + 128 * s, 65536 + sl * STRB + q * 1024);
    }
    issueF(s, 0);
  };
  issue(H, false); issueF(H, 1);
  BAR();
  issue(H + 2, true);
  if (H == 0) { issueF(2, 1); WAIT_V(NP); } else WAIT_V(NP - 4 * NCG);
  lds_st1(M_FULL + H * 4, H + 1);
  if (H) { spin_prog(1); issueF(3, 1); }
#pragma unroll 1
  for (int s = H + 4; s < 10; s += 2) {
    spin_prog(2 * s - 6);
    TS(20 + s);
    issue(s, true);
    TS(30 + s);
    spin_prog(2 * s - 5);
    issueF(s, 1);
    TS(40 + s);
    WAIT_V(NP);
    lds_st1(M_FULL + H * 4, s - 1);
    TS(50 + s);
  }
  WAIT_V(0);
  lds_st1(M_FULL + H * 4, H + 9);
}
template <int ROLE>
__device__ __forceinline__ void loader2(const Params& P, const Geo& G, int H, int lane, int ct, int q0) {
  constexpr int NQ = ROLE ? 10 : 12;
  const int r8 = lane >> 3, c8 = lane & 7;
  const rsrc_t r0 = ROLE ? mk_rsrc(P.Cre, 640 * 1280) : mk_rsrc(P.w2, 1536 * 1280), r1 = ROLE ? mk_rsrc(P.Cim, 640 * 1280) : r0;
  int vo[5];
  if (ROLE) vo[0] = r8 * 1280 + ((c8 ^ r8) * 16);
  else {
#pragma unroll
    for (int v = 0; v < 5; ++v) { const int t = 64 * v + lane; vo[v] = (t / 10) * 1280 + (t % 10) * 16; }
  }
  auto issue = [&](int q) {
    const int qq = q + q0 >= NQ ? q + q0 - NQ : q + q0;
    const int sl = (q >= 7 ? q - 7 : q) * SLOT2;
#pragma unroll
    for (int j = 0; j < 20; ++j) {
      if (ROLE) dma(j >= 10 ? r1 : r0, vo[0], (80 * ct + 8 * (j % 10)) * 1280 + 128 * qq, sl + j * 1024);
      else dma(r0, vo[j % 5], (128 * qq + 32 * (j / 5)) * 1280 + 160 * ct, sl + j * 1024);
    }
  };
  spin_prog(20);
  issue(H); issue(H + 2); issue(H + 4);
#pragma unroll 1
  for (int q = H; q + 6 < NQ; q += 2) {
    WAIT_V(40);
    lds_st1(M_FULL + H * 4, 11 + q);
    if (q > 0) spin_prog(20 + q);
    issue(q + 6);
  }
  WAIT_V(40);
  lds_st1(M_FULL + H * 4, 11 + NQ - 6 + H);
  WAIT_V(20);
  lds_st1(M_FULL + H * 4, 11 + NQ - 4 + H);
  WAIT_V(0);
  lds_st1(M_FULL + H * 4, 11 + NQ - 2 + H);
}

__device__ __forceinline__ unsigned read_tag(rsrc_t rs) {
  return __builtin_amdgcn_readfirstlane(__builtin_amdgcn_raw_buffer_load_b32(rs, W_EPOCH, 0, 16)) + 1u;
}
__device__ __forceinline__ int chpos(int lc) {
  const int w = lc & 15;
  return (lc & 48) + 4 * (2 * ((w >> 2) & 1) + (w >> 3)) + (w & 3);
}
__device__ __forceinline__ void service_h2(const Params& P, const Geo& G, rsrc_t rs, int lane, int role) {
  const int r8 = lane >> 3, c8 = lane & 7;
  const unsigned tag = read_tag(rs);
  u32x4 v[10];
  {
    const GAS char* src = (const GAS char*)P.x + (size_t)(128 * G.c + 8 * G.pid + r8) * 1280 + c8 * 16;
#pragma unroll
    for (int q = 0; q < 10; ++q) v[q] = *(const GAS u32x4*)(src + q * 128);
  }
  BAR();
  {
    float s = 0.f;
#pragma unroll
    for (int q = 0; q < 10; ++q) s += sum8(v[q]);
    s += __shfl_xor(s, 1); s += __shfl_xor(s, 2); s += __shfl_xor(s, 4);
    const float mean = s * (1.f / 640.f);
    float ss = 0.f;
#pragma unroll
    for (int q = 0; q < 10; ++q)
#pragma unroll
      for (int e = 0; e < 4; ++e) { const float a = lo_f(v[q][e]) - mean, b = hi_f(v[q][e]) - mean; ss += a * a + b * b; }
    ss += __shfl_xor(ss, 1); ss += __shfl_xor(ss, 2); ss += __shfl_xor(ss, 4);
    const float mb = bfr(mean);
    const float den = bfr(sqrtf(bfr(bfr(ss * (1.f / 640.f)) + 1e-5f)));
    if (c8 == 0) {
      u32x4 o = {__float_as_uint(mb), tag, __float_as_uint(1.f / den), tag};
      st_sc1(o, rs, W_STAT + (128 * G.c + 8 * G.pid + r8) * 16, 0);
    }
  }
  TS(1);
  const int nch = G.type ? 32 : 48, chb = G.type ? 384 + 32 * G.a : 48 * G.a;
  const int ncol = G.type ? 128 : 64, colb = G.type ? 512 + 128 * G.a : 64 * G.a;
  const int vs = W_STAT + (128 * G.c + lane) * 16;
  const int vb = W_CSB + (chb + (lane < nch ? lane : 0)) * 16;
  const int vf = W_CSF + (colb + (2 * lane < ncol ? 2 * lane : 0)) * 8;
  u32x4 s0, s1, cb, cf[10];
#pragma clang loop unroll(disable)
  for (unsigned sp = 0; sp < SPIN_CAP; ++sp) {
    __builtin_amdgcn_s_sleep(8);
    MEMBAR();
    s0 = ld_sc1(rs, vs, 0); s1 = ld_sc1(rs, vs + 1024, 0); cb = ld_sc1(rs, vb, 0);
#pragma unroll
    for (int k = 0; k < 10; ++k) cf[k] = ld_sc1(rs, vf + k * 12288, 0);
    bool ok = (s0[1] == tag) & (s0[3] == tag) & (s1[1] == tag) & (s1[3] == tag) & (cb[1] == tag) & (cb[3] == tag);
#pragma unroll
    for (int k = 0; k < 10; ++k) ok &= (cf[k][1] == tag) & (cf[k][3] == tag);
    if (__all(ok)) break;
  }
  *(LAS u32x2*)(size_t)(M_STAT + lane * 8) = (u32x2){s0[0], s0[2]};
  *(LAS u32x2*)(size_t)(M_STAT + 512 + lane * 8) = (u32x2){s1[0], s1[2]};
  if (lane < nch) {
    *(LAS unsigned*)(size_t)(M_CS + chpos(lane) * 4) = cb[0];
    *(LAS unsigned*)(size_t)(M_CS + 192 + chpos(lane) * 4) = cb[2];
  }
  if (2 * lane < ncol) {
    float c0 = 0.f, c1 = 0.f;
#pragma unroll
    for (int k = 0; k < 10; ++k) { c0 += __uint_as_float(cf[k][0]); c1 += __uint_as_float(cf[k][2]); }
    *(LAS f32x2*)(size_t)(M_CS + (96 + 2 * lane) * 4) = (f32x2){c0, c1};
  }
  WAIT_L(0);
  lds_st1(M_SVCA, 1u);
  TS(2);
  const int vfl = (role ? W_FLAGH : W_FLAGA) + G.c * 512 + lane * 8;
  spin_ge(role ? M_CARRY : M_CNTE, role ? 1u : 4u);
#pragma clang loop unroll(disable)
  for (unsigned sp = 0; sp < SPIN_CAP; ++sp) {
    const u32x2 f = __builtin_amdgcn_raw_buffer_load_b64(rs, vfl, 0, 16);
    if (__all((f[0] == tag) & (f[1] == ~tag))) break;
    __builtin_amdgcn_s_sleep(2);
    MEMBAR();
  }
  MEMBAR();
  lds_st1(M_GO, 1u);
  TS(3);
}
__device__ __forceinline__ void service_h3(const Params& P, const Geo& G, rsrc_t rs, int lane) {
  const int r8 = lane >> 3, c8 = lane & 7;
  const unsigned tag = read_tag(rs);
  const int nch = G.type ? 32 : 48, chb = G.type ? 384 + 32 * G.a : 48 * G.a;
  const int jb = G.u + 128 * (r8 >> 1);
  const bool bok = (r8 < 6) && (jb < 320);
  const int jc = bok ? jb : 0;
  const int chs = (jc < 192) ? jc + 192 * G.par : 384 + (jc - 192) + 128 * G.par;
  u32x4 v[10], f[8];
  {
    const GAS char* src = (const GAS char*)((r8 & 1) ? P.Bim : P.Bre) + (size_t)chs * 1280 + c8 * 16;
#pragma unroll
    for (int q = 0; q < 10; ++q) v[q] = *(const GAS u32x4*)(src + q * 128);
  }
  const int uu = G.u < 120 ? G.u : 0;
  const int cgi = uu % 12, kr = uu / 12;
  const int cg = (cgi < 4) ? cgi + 4 * G.par : 8 + (cgi - 4) + 8 * G.par;
  {
    const GAS char* src = (const GAS char*)P.w1 + (size_t)(64 * kr + r8) * 3072 + cg * 128 + c8 * 16;
#pragma unroll
    for (int q = 0; q < 8; ++q) f[q] = *(const GAS u32x4*)(src + q * 8 * 3072);
  }
  const int chp = chb + (lane < nch ? lane : 0);
  const unsigned short wl = *(const GAS unsigned short*)((const GAS char*)P.logdt + chp * 2);
  const unsigned short wr = *(const GAS unsigned short*)((const GAS char*)P.lre + chp * 2);
  const unsigned short wi = *(const GAS unsigned short*)((const GAS char*)P.lim + chp * 2);
  BAR();
  {
    float s = 0.f;
#pragma unroll
    for (int q = 0; q < 10; ++q) s += sum8(v[q]);
    s += __shfl_xor(s, 1); s += __shfl_xor(s, 2); s += __shfl_xor(s, 4);
    const float si = __shfl_xor(s, 8);
    if (bok && (lane & 15) == 0) {
      u32x4 o = {__float_as_uint(s), tag, __float_as_uint(si), tag};
      st_sc1(o, rs, W_CSB + chs * 16, 0);
    }
    float cs[8];
#pragma unroll
    for (int e = 0; e < 4; ++e) {
      float a = 0.f, b = 0.f;
#pragma unroll
      for (int q = 0; q < 8; ++q) { a += lo_f(f[q][e]); b += hi_f(f[q][e]); }
      cs[2 * e] = a; cs[2 * e + 1] = b;
    }
#pragma unroll
    for (int e = 0; e < 8; ++e) { cs[e] += __shfl_xor(cs[e], 8); cs[e] += __shfl_xor(cs[e], 16); cs[e] += __shfl_xor(cs[e], 32); }
    const int rs4 = r8 & 3;
    const float o0 = rs4 == 0 ? cs[0] : rs4 == 1 ? cs[2] : rs4 == 2 ? cs[4] : cs[6];
    const float o1 = rs4 == 0 ? cs[1] : rs4 == 1 ? cs[3] : rs4 == 2 ? cs[5] : cs[7];
    if (G.u < 120 && r8 < 4) {
      u32x4 o = {__float_as_uint(o0), tag, __float_as_uint(o1), tag};
      st_sc1(o, rs, W_CSF + kr * 12288 + (64 * cg + 8 * c8 + 2 * rs4) * 8, 0);
    }
  }
  TS(1);
  f32x2 L64, L128;
  const int cl = lane < nch ? lane : 0, pos = chpos(cl);
  {
    f32x2 L, CF;
    const float dt = bfr(expf(bf2f(wl)));
    const float ar = bf2f(wr), ai = bf2f(wi);
    const float zr = dt * ar, zi = dt * ai;
    const float ex = expf(zr);
    L.x = ex * cosf(zi); L.y = ex * sinf(zi);
    const float nr = L.x - 1.f, ni = L.y, dd = ar * ar + ai * ai;
    CF.x = (nr * ar + ni * ai) / dd; CF.y = (ni * ar - nr * ai) / dd;
    const f32x2 L2 = cmul(L, L), L3 = cmul(L2, L), L4 = cmul(L2, L2), L8 = cmul(L4, L4), L16 = cmul(L8, L8), L32 = cmul(L16, L16);
    L64 = cmul(L32, L32); L128 = cmul(L64, L64);
    const f32x2 tb[10] = {L, L4, L8, L16, L32, CF, cmul(CF, L), cmul(CF, L2), cmul(CF, L3), cmul(CF, L4)};
    if (lane < nch) {
#pragma unroll
      for (int k = 0; k < 10; ++k) {
        *(LAS float*)(size_t)(M_TAB + k * 384 + pos * 4) = tb[k].x;
        *(LAS float*)(size_t)(M_TAB + k * 384 + 192 + pos * 4) = tb[k].y;
      }
    }
    lds_st1(M_TAG, tag);
    WAIT_L(0);
    lds_st1(M_SVCB, 1u);
  }
  TS(2);
  spin_ge(M_CNTE, 4u);
  const f32x2 e0 = {*(const LAS float*)(size_t)(M_ENDW + pos * 4), *(const LAS float*)(size_t)(M_ENDW + 192 + pos * 4)};
  const f32x2 e1 = {*(const LAS float*)(size_t)(M_ENDW + 384 + pos * 4), *(const LAS float*)(size_t)(M_ENDW + 576 + pos * 4)};
  const int ve = W_EGR + (chb + cl) * 16;
  if (G.c < 15 && lane < nch) {
    const f32x2 ec = cfma(L64, e0, e1);
    u32x4 o = {__float_as_uint(ec.x), tag, __float_as_uint(ec.y), tag};
    st_sc1(o, rs, ve + G.c * 10240, 0);
  }
  f32x2 S = {0.f, 0.f};
  if (G.c > 0) {
    u32x4 ev[15];
#pragma clang loop unroll(disable)
    for (unsigned sp = 0; sp < SPIN_CAP; ++sp) {
      bool ok = true;
#pragma unroll
      for (int j = 0; j < 15; ++j) ev[j] = ld_sc1(rs, ve + (j < G.c ? j : 0) * 10240, 0);
#pragma unroll
      for (int j = 0; j < 15; ++j) ok &= (ev[j][1] == tag) & (ev[j][3] == tag);
      if (__all(ok)) break;
      __builtin_amdgcn_s_sleep(1);
      MEMBAR();
    }
#pragma unroll
    for (int j = 0; j < 15; ++j)
      if (j < G.c) S = cfma(L128, S, (f32x2){__uint_as_float(ev[j][0]), __uint_as_float(ev[j][2])});
  }
  if (lane < nch) {
    const f32x2 S1 = cfma(L64, S, e0);
    *(LAS float*)(size_t)(M_SIN + pos * 4) = S.x; *(LAS float*)(size_t)(M_SIN + 192 + pos * 4) = S.y;
    *(LAS float*)(size_t)(M_SIN + 384 + pos * 4) = S1.x; *(LAS float*)(size_t)(M_SIN + 576 + pos * 4) = S1.y;
  }
  WAIT_L(0);
  lds_st1(M_CARRY, 1u);
  TS(3);
}

__global__ __launch_bounds__(512, 2) void fused_kernel(const Params P) {
  __shared__ __attribute__((aligned(1024))) char lds_[163840];
  asm volatile("" ::"v"((LAS char*)lds_) : "memory");
  const int tid = threadIdx.x, wid = __builtin_amdgcn_readfirstlane(tid >> 6), lane = tid & 63;
  const int b = blockIdx.x, xcd = b & 7, r = b >> 3;
  Geo G;
  G.c = 4 * (xcd >> 1) + (r & 3); G.jj = r >> 2; G.type = G.jj & 1; G.par = xcd & 1;
  G.a = 4 * G.par + (G.jj >> 1); G.pid = 8 * G.par + G.jj; G.u = (xcd >> 1) * 32 + r;
  const int role = G.jj >> 2, ct = 4 * G.par + (G.jj & 3);
  const int q0 = role ? ((G.jj & 3) * 10) >> 2 : 3 * (G.jj & 3);
  const rsrc_t rs = mk_rsrc(P.ws, W_END);
  TS(0); TC(62);
  if (wid < 4) {
    if (wid == 0) {
      if (lane < 16) *(LAS unsigned*)(size_t)(MISC + lane * 4) = 0u;
      WAIT_L(0);
    }
    const int wn = wid & 1;
    if (G.type == 0 && wn == 0) p1_mfma<2, 0>(P, G, rs, wid, lane, 0, 114688, 12288, 4096, 0, 48 + 3 * G.a, 96);
    else {
      const int pair0 = G.type ? wn : 2;
      const int offF = G.type ? 98304 + wn * 4096 : 114688;
      const int fstep0 = G.type ? 16 + 4 * G.a + 2 * wn : 2 * G.a;
      const int hstep0 = G.type ? 72 + 2 * G.a + wn : 48 + 3 * G.a + 2;
      const int csf0 = G.type ? 96 + 64 * wn : 96;
      p1_mfma<1, 2>(P, G, rs, wid, lane, pair0, offF, G.type ? 8192 : 12288, G.type ? 8192 : 4096, fstep0, hstep0, csf0);
    }
    if (role) p2_mfma<1>(P, G, rs, wid, lane, ct, q0);
    else p2_mfma<0>(P, G, rs, wid, lane, ct, q0);
  } else if (wid < 6) {
    const int H = wid - 4;
    if (G.type) loader1<1>(P, G, H, lane); else loader1<0>(P, G, H, lane);
    if (role) loader2<1>(P, G, H, lane, ct, q0); else loader2<0>(P, G, H, lane, ct, q0);
    if (b == 0 && H == 0) {
      const unsigned tag = lds_ld(M_TAG);
#pragma clang loop unroll(disable)
      for (unsigned sp = 0; sp < SPIN_CAP; ++sp) {
        bool ok = true;
#pragma unroll
        for (int k = 0; k < 4; ++k) {
          const u32x2 f = __builtin_amdgcn_raw_buffer_load_b64(rs, W_FLAGH + (lane * 4 + k) * 32, 0, 16);
          ok &= (f[0] == tag) & (f[1] == ~tag);
        }
        if (__all(ok)) break;
        __builtin_amdgcn_s_sleep(2);
        MEMBAR();
      }
      if (lane == 0) __builtin_amdgcn_raw_buffer_store_b32(tag, rs, W_EPOCH, 0, 16);
    }
  } else if (wid == 6) {
    service_h2(P, G, rs, lane, role);
  } else {
    service_h3(P, G, rs, lane);
  }
  TS(9); TC(63);
}

extern "C" void kernel_launch(void* const* d_in, const int* in_sizes, int n_in, void* d_out, int out_size, void* d_ws,
                              size_t ws_size, hipStream_t stream) {
  Params P;
  P.x = (const char*)d_in[0]; P.lre = (const char*)d_in[1]; P.lim = (const char*)d_in[2];
  P.Bre = (const char*)d_in[3]; P.Bim = (const char*)d_in[4]; P.Cre = (const char*)d_in[5]; P.Cim = (const char*)d_in[6];
  P.logdt = (const char*)d_in[7]; P.dskip = (const char*)d_in[8]; P.w1 = (const char*)d_in[10]; P.w2 = (const char*)d_in[11];
  P.ws = (char*)d_ws; P.out = (float*)d_out;
  fused_kernel<<<256, 512, 0, stream>>>(P);
}
```

```cpp
#include <hip/hip_runtime.h>
#include <hip/hip_bf16.h>
#include <stdint.h>

typedef __attribute__((ext_vector_type(8))) short bf16x8;
typedef __attribute__((ext_vector_type(4))) short s16x4;
typedef __attribute__((ext_vector_type(4))) float f32x4;
typedef __attribute__((ext_vector_type(2))) float f32x2;
typedef __attribute__((ext_vector_type(4))) unsigned u32x4;
typedef __attribute__((ext_vector_type(2))) unsigned u32x2;
typedef __attribute__((ext_vector_type(2))) __bf16 bf2_t;
#define GAS __attribute__((address_space(1)))
#define LAS __attribute__((address_space(3)))
typedef LAS char* lptr;

#define WAIT_V(n) asm volatile("s_waitcnt vmcnt(%0)" ::"n"(n) : "memory")
#define WAIT_L(n) asm volatile("s_waitcnt lgkmcnt(%0)" ::"n"(n) : "memory")
#define SCHED() __builtin_amdgcn_sched_barrier(0)
#define MEMBAR() asm volatile("" ::: "memory")
#define BAR() do { MEMBAR(); __builtin_amdgcn_s_barrier(); MEMBAR(); } while (0)

constexpr int W_EPOCH = 0;
constexpr int W_FLAGA = 4096;
constexpr int W_FLAGH = 12288;
constexpr int W_STAT  = 20480;
constexpr int W_CSB   = 53248;
constexpr int W_CSF   = 65536;
constexpr int W_EGR   = 196608;
constexpr int W_AF    = 393216;
constexpr int W_GB    = W_AF + 16 * 8 * 88 * 1024;
constexpr int W_END   = W_GB + 16 * 4 * 40 * 2048;
constexpr int W_DBG   = W_END;
constexpr int MISC   = 155648;
constexpr int M_FULL = MISC;
constexpr int M_PROG = MISC + 16;
constexpr int M_SVCA = MISC + 32, M_SVCB = MISC + 36, M_CNTE = MISC + 40, M_CARRY = MISC + 44, M_GO = MISC + 48, M_TAG = MISC + 52;
constexpr int M_STAT = MISC + 256;
constexpr int M_CS   = MISC + 1280;
constexpr int M_TAB  = MISC + 2304;
constexpr int M_ENDW = MISC + 6144;
constexpr int M_SIN  = MISC + 6912;
constexpr int M_PW   = 147456;
constexpr int SLOT2  = 20480;
constexpr unsigned SPIN_CAP = 1u << 16;
#define L2_DEPTH 2

__device__ __forceinline__ float bf2f(unsigned short v) { return __uint_as_float((unsigned)v << 16); }
__device__ __forceinline__ float bfr(float a) { return (float)(__bf16)a; }
__device__ __forceinline__ unsigned pack2(float a, float b) {
  bf2_t v = {(__bf16)a, (__bf16)b};
  return __builtin_bit_cast(unsigned, v);
}
__device__ __forceinline__ float lo_f(unsigned u) { return __uint_as_float(u << 16); }
__device__ __forceinline__ float hi_f(unsigned u) { return __uint_as_float(u & 0xFFFF0000u); }
__device__ __forceinline__ f32x2 cmul(f32x2 a, f32x2 b) {
  f32x2 r; r.x = a.x * b.x - a.y * b.y; r.y = a.x * b.y + a.y * b.x; return r;
}
__device__ __forceinline__ f32x2 cfma(f32x2 a, f32x2 b, f32x2 c) {
  f32x2 r; r.x = fmaf(a.x, b.x, fmaf(-a.y, b.y, c.x)); r.y = fmaf(a.x, b.y, fmaf(a.y, b.x, c.y)); return r;
}
struct C4 { f32x4 re, im; };
__device__ __forceinline__ f32x4 fma4(f32x4 a, f32x4 b, f32x4 c) { return __builtin_elementwise_fma(a, b, c); }
__device__ __forceinline__ C4 cfma4(const C4& a, const C4& b, const C4& c) {
  C4 r; r.re = fma4(a.re, b.re, fma4(-a.im, b.im, c.re)); r.im = fma4(a.re, b.im, fma4(a.im, b.re, c.im)); return r;
}
__device__ __forceinline__ C4 cmul4(const C4& a, const C4& b) {
  C4 r; r.re = fma4(a.re, b.re, -(a.im * b.im)); r.im = fma4(a.re, b.im, a.im * b.re); return r;
}
template <int N> __device__ __forceinline__ float row_shr(float v) {
  return __builtin_bit_cast(float, __builtin_amdgcn_update_dpp(0, __builtin_bit_cast(int, v), 0x110 + N, 0xf, 0xf, true));
}
template <int N> __device__ __forceinline__ C4 row_shr4(const C4& v) {
  C4 r;
#pragma unroll
  for (int e = 0; e < 4; ++e) { r.re[e] = row_shr<N>(v.re[e]); r.im[e] = row_shr<N>(v.im[e]); }
  return r;
}
__device__ __forceinline__ u32x2 silu_pack4(f32x4 v) {
  const unsigned p0 = pack2(v[0], v[1]), p1 = pack2(v[2], v[3]);
  const f32x4 x = {lo_f(p0), hi_f(p0), lo_f(p1), hi_f(p1)};
  const f32x4 t = x * (-1.44269504088896341f);
  f32x4 d;
#pragma unroll
  for (int e = 0; e < 4; ++e) d[e] = __builtin_amdgcn_exp2f(t[e]);
  d = d + 1.f;
#pragma unroll
  for (int e = 0; e < 4; ++e) d[e] = __builtin_amdgcn_rcpf(d[e]);
  const f32x4 s = x * d;
  return (u32x2){pack2(s[0], s[1]), pack2(s[2], s[3])};
}
__device__ __forceinline__ float sum8(u32x4 v) {
  return ((lo_f(v[0]) + hi_f(v[0])) + (lo_f(v[1]) + hi_f(v[1]))) + ((lo_f(v[2]) + hi_f(v[2])) + (lo_f(v[3]) + hi_f(v[3])));
}
typedef __amdgpu_buffer_rsrc_t rsrc_t;
__device__ __forceinline__ rsrc_t mk_rsrc(const void* p, int bytes) { return __builtin_amdgcn_make_buffer_rsrc((void*)p, 0, bytes, 0x00020000); }
__device__ __forceinline__ void dma(rsrc_t r, int voff, int soff, unsigned l) {
  __builtin_amdgcn_raw_ptr_buffer_load_lds(r, (LAS void*)(size_t)l, 16, voff, soff, 0, 0);
}
__device__ __forceinline__ bf16x8 lds_b128(unsigned a) { return *(const LAS bf16x8*)(size_t)a; }
__device__ __forceinline__ f32x4 lds_f4(unsigned a) { return *(const LAS f32x4*)(size_t)a; }
__device__ __forceinline__ s16x4 lds_tr(unsigned a) { return __builtin_amdgcn_ds_read_tr16_b64_v4i16((LAS s16x4*)(size_t)a); }
__device__ __forceinline__ bf16x8 cat8(s16x4 a, s16x4 b) { return __builtin_shufflevector(a, b, 0, 1, 2, 3, 4, 5, 6, 7); }
__device__ __forceinline__ unsigned opaque(unsigned v) { asm volatile("" : "+v"(v)); return v; }
__device__ __forceinline__ u32x4 ld_sc1(rsrc_t r, int voff, int soff) { return __builtin_amdgcn_raw_buffer_load_b128(r, voff, soff, 16); }
__device__ __forceinline__ void st_sc1(u32x4 v, rsrc_t r, int voff, int soff) { __builtin_amdgcn_raw_buffer_store_b128(v, r, voff + soff, 0, 16); }
__device__ __forceinline__ unsigned lds_ld(unsigned a) { return *(volatile LAS unsigned*)(size_t)a; }
__device__ __forceinline__ void lds_st1(unsigned a, unsigned v) {
  asm volatile("s_mov_b64 exec, 1\n\tds_write_b32 %0, %1\n\ts_mov_b64 exec, -1" ::"v"(a), "v"(v) : "memory");
}
__device__ __forceinline__ void spin_ge(unsigned a, unsigned need) {
#pragma clang loop unroll(disable)
  for (unsigned s = 0; s < (1u << 22); ++s) {
    if (__builtin_amdgcn_readfirstlane(lds_ld(a)) >= need) break;
    __builtin_amdgcn_s_sleep(1);
  }
  MEMBAR();
}
__device__ __forceinline__ unsigned min4(u32x4 p) {
  const unsigned a = p[0] < p[1] ? p[0] : p[1], b = p[2] < p[3] ? p[2] : p[3];
  return a < b ? a : b;
}
__device__ __forceinline__ void spin_ge4(unsigned a, unsigned need) {
#pragma clang loop unroll(disable)
  for (unsigned s = 0; s < (1u << 18); ++s) {
    if (__builtin_amdgcn_readfirstlane(min4(*(volatile LAS u32x4*)(size_t)a)) >= need) break;
    __builtin_amdgcn_s_sleep(1);
  }
  MEMBAR();
}
__device__ __forceinline__ float dsum2(unsigned v, float c) {
  const bf2_t one = {(__bf16)1.f, (__bf16)1.f};
  return __builtin_amdgcn_fdot2_f32_bf16(__builtin_bit_cast(bf2_t, v), one, c, false);
}
__device__ __forceinline__ unsigned read_prog() {
  const u32x4 p = *(volatile LAS u32x4*)(size_t)M_PROG;
  const unsigned a = p[0] < p[1] ? p[0] : p[1], b = p[2] < p[3] ? p[2] : p[3];
  return __builtin_amdgcn_readfirstlane(a < b ? a : b);
}
__device__ __forceinline__ void spin_prog(unsigned need) {
#pragma clang loop unroll(disable)
  for (unsigned s = 0; s < (1u << 22); ++s) {
    const u32x4 p = *(volatile LAS u32x4*)(size_t)M_PROG;
    const unsigned a = p[0] < p[1] ? p[0] : p[1], b = p[2] < p[3] ? p[2] : p[3];
    if (__builtin_amdgcn_readfirstlane(a < b ? a : b) >= need) break;
    __builtin_amdgcn_s_sleep(1);
  }
  MEMBAR();
}
__device__ __forceinline__ void lds_inc(unsigned a) {
  if ((threadIdx.x & 63) == 0) __hip_atomic_fetch_add((LAS unsigned*)(size_t)a, 1u, __ATOMIC_RELAXED, __HIP_MEMORY_SCOPE_WORKGROUP);
}
#define TS(k) do {} while (0)
#define TC(k) do {} while (0)

struct Params {
  const char *x, *lre, *lim, *Bre, *Bim, *Cre, *Cim, *logdt, *dskip, *w1, *w2;
  char* ws;
  float* out;
};
struct Geo {
  int c, type, a, pid, par, u, jj;
};

__device__ __forceinline__ unsigned ld_epoch(rsrc_t rs) { return __builtin_amdgcn_raw_buffer_load_b32(rs, W_EPOCH, 0, 16); }
__device__ __forceinline__ int chpos(int lc) {
  const int w = lc & 15;
  return (lc & 48) + 4 * (2 * ((w >> 2) & 1) + (w >> 3)) + (w & 3);
}
__device__ __forceinline__ void svc_rowstat(const Params& P, Geo& G, rsrc_t rs, int lane) {
  const int r8 = lane >> 3, c8 = lane & 7;
  const unsigned ep = ld_epoch(rs);
  u32x4 v[10];
  const GAS char* src = (const GAS char*)P.x + (size_t)(128 * G.c + 8 * G.pid + r8) * 1280 + c8 * 16;
#pragma unroll
  for (int q = 0; q < 10; ++q) v[q] = *(const GAS u32x4*)(src + q * 128);
  if (lane < 16) *(LAS unsigned*)(size_t)(MISC + lane * 4) = 0u;
  WAIT_L(0);
  BAR();
  float s0 = 0.f, s1 = 0.f;
#pragma unroll
  for (int q = 0; q < 10; ++q) { s0 = dsum2(v[q][0], s0); s1 = dsum2(v[q][1], s1); s0 = dsum2(v[q][2], s0); s1 = dsum2(v[q][3], s1); }
  float s = s0 + s1;
  s += __shfl_xor(s, 1); s += __shfl_xor(s, 2); s += __shfl_xor(s, 4);
  const float mean = s * (1.f / 640.f);
  f32x2 q0 = {0.f, 0.f}, q1 = {0.f, 0.f};
#pragma unroll
  for (int q = 0; q < 10; ++q)
#pragma unroll
    for (int e = 0; e < 4; e += 2) {
      const f32x2 a = (f32x2){lo_f(v[q][e]), hi_f(v[q][e])} - mean, b = (f32x2){lo_f(v[q][e + 1]), hi_f(v[q][e + 1])} - mean;
      q0 = __builtin_elementwise_fma(a, a, q0); q1 = __builtin_elementwise_fma(b, b, q1);
    }
  float ss = (q0[0] + q0[1]) + (q1[0] + q1[1]);
  ss += __shfl_xor(ss, 1); ss += __shfl_xor(ss, 2); ss += __shfl_xor(ss, 4);
  const float mb = bfr(mean);
  const float den = bfr(sqrtf(bfr(bfr(ss * (1.f / 640.f)) + 1e-5f)));
  if (c8 == 0) {
    const unsigned tag = ep + 1u;
    st_sc1((u32x4){__float_as_uint(mb), tag, __float_as_uint(1.f / den), tag}, rs, W_STAT + (128 * G.c + 8 * G.pid + r8) * 16, 0);
  }
}
__device__ __forceinline__ void svc_colsum(const Params& P, Geo& G, rsrc_t rs, int lane) {
  const int r8 = lane >> 3, c8 = lane & 7;
  const unsigned ep = ld_epoch(rs);
  const int jb = G.u + 128 * (r8 >> 1);
  const bool bok = (r8 < 6) && (jb < 320);
  const int jc = bok ? jb : 0;
  const int chs = (jc < 192) ? jc + 192 * G.par : 384 + (jc - 192) + 128 * G.par;
  u32x4 v[10], f[8];
  {
    const GAS char* src = (const GAS char*)((r8 & 1) ? P.Bim : P.Bre) + (size_t)chs * 1280 + c8 * 16;
#pragma unroll
    for (int q = 0; q < 10; ++q) v[q] = *(const GAS u32x4*)(src + q * 128);
  }
  const int uu = G.u < 120 ? G.u : 0;
  const int cgi = uu % 12, kr = uu / 12;
  const int cg = (cgi < 4) ? cgi + 4 * G.par : 8 + (cgi - 4) + 8 * G.par;
  {
    const GAS char* src = (const GAS char*)P.w1 + (size_t)(64 * kr + r8) * 3072 + cg * 128 + c8 * 16;
#pragma unroll
    for (int q = 0; q < 8; ++q) f[q] = *(const GAS u32x4*)(src + q * 8 * 3072);
  }
  BAR();
  const unsigned tag = ep + 1u;
  float s0 = 0.f, s1 = 0.f;
#pragma unroll
  for (int q = 0; q < 10; ++q) { s0 = dsum2(v[q][0], s0); s1 = dsum2(v[q][1], s1); s0 = dsum2(v[q][2], s0); s1 = dsum2(v[q][3], s1); }
  float s = s0 + s1;
  s += __shfl_xor(s, 1); s += __shfl_xor(s, 2); s += __shfl_xor(s, 4);
  const float si = __shfl_xor(s, 8);
  float cs[8];
#pragma unroll
  for (int e = 0; e < 4; ++e) {
    f32x2 a = {0.f, 0.f};
#pragma unroll
    for (int q = 0; q < 8; ++q) a += (f32x2){lo_f(f[q][e]), hi_f(f[q][e])};
    cs[2 * e] = a[0]; cs[2 * e + 1] = a[1];
  }
#pragma unroll
  for (int e = 0; e < 8; ++e) { cs[e] += __shfl_xor(cs[e], 8); cs[e] += __shfl_xor(cs[e], 16); cs[e] += __shfl_xor(cs[e], 32); }
  const int rs4 = r8 & 3;
  const float o0 = rs4 == 0 ? cs[0] : rs4 == 1 ? cs[2] : rs4 == 2 ? cs[4] : cs[6];
  const float o1 = rs4 == 0 ? cs[1] : rs4 == 1 ? cs[3] : rs4 == 2 ? cs[5] : cs[7];
  if (bok && (lane & 15) == 0) st_sc1((u32x4){__float_as_uint(s), tag, __float_as_uint(si), tag}, rs, W_CSB + chs * 16, 0);
  if (G.u < 120 && r8 < 4) st_sc1((u32x4){__float_as_uint(o0), tag, __float_as_uint(o1), tag}, rs, W_CSF + kr * 12288 + (64 * cg + 8 * c8 + 2 * rs4) * 8, 0);
}
template <int PART>
__device__ __forceinline__ void svc_tables(const Params& P, Geo& G, rsrc_t rs, int lane) {
  const int nch = G.type ? 32 : 48, chb = G.type ? 384 + 32 * G.a : 48 * G.a;
  const int cl = lane < nch ? lane : 0, pos = chpos(cl), chp = chb + cl;
  const unsigned short wl = *(const GAS unsigned short*)((const GAS char*)P.logdt + chp * 2);
  const unsigned short wr = *(const GAS unsigned short*)((const GAS char*)P.lre + chp * 2);
  const unsigned short wi = *(const GAS unsigned short*)((const GAS char*)P.lim + chp * 2);
  unsigned ep = 0;
  if (PART) ep = ld_epoch(rs);
  BAR();
  const float dt = bfr(expf(bf2f(wl)));
  const float ar = bf2f(wr), ai = bf2f(wi);
  const float zr = dt * ar, zi = dt * ai;
  const float ex = expf(zr);
  float sn, cs;
  sincosf(zi, &sn, &cs);
  const f32x2 L = {ex * cs, ex * sn};
  const f32x2 L2 = cmul(L, L), L4 = cmul(L2, L2);
  f32x2 tb[5];
  if (PART == 0) {
    tb[0] = L; tb[1] = L4; tb[2] = cmul(L4, L4); tb[3] = cmul(tb[2], tb[2]); tb[4] = cmul(tb[3], tb[3]);
  } else {
    const float nr = L.x - 1.f, ni = L.y, dd = ar * ar + ai * ai;
    const f32x2 CF = {(nr * ar + ni * ai) / dd, (ni * ar - nr * ai) / dd};
    tb[0] = CF; tb[1] = cmul(CF, L); tb[2] = cmul(CF, L2); tb[3] = cmul(CF, cmul(L2, L)); tb[4] = cmul(CF, L4);
  }
  if (lane < nch) {
#pragma unroll
    for (int k = 0; k < 5; ++k) {
      *(LAS float*)(size_t)(M_TAB + (5 * PART + k) * 384 + pos * 4) = tb[k].x;
      *(LAS float*)(size_t)(M_TAB + (5 * PART + k) * 384 + 192 + pos * 4) = tb[k].y;
    }
  }
  if (PART) lds_st1(M_TAG, ep + 1u);
  WAIT_L(0);
  lds_inc(M_SVCB);
}

template <int NB, int NF, bool RD, bool CHK, bool PRE>
__device__ __forceinline__ void p1_half(f32x4 (&acc)[4][2 * NB + 2 * NF], const bf16x8 (&ca)[4], const bf16x8 (&cw)[2 * NB + 2 * NF],
                                        bf16x8 (&na)[4], bf16x8 (&nw)[2 * NB + 2 * NF], unsigned vA, unsigned vB, const unsigned (&aF)[4],
                                        unsigned fo, u32x4& fl, unsigned need, unsigned vprog, unsigned prog) {
  constexpr int NT = 2 * NB + 2 * NF;
#pragma unroll
  for (int j = 0; j < 4 * NT; ++j) {
    acc[j & 3][j >> 2] = __builtin_amdgcn_mfma_f32_16x16x32_bf16(cw[j >> 2], ca[j & 3], acc[j & 3][j >> 2], 0, 0, 0);
    SCHED();
    if (j == 1) {
      WAIT_L(0);
      unsigned z = 0;
      if (CHK) z = __builtin_amdgcn_readfirstlane(min4(fl));
      SCHED();
      lds_st1(vprog, prog);
      if (CHK) { if (z < need) spin_ge4(M_FULL, need); }
      SCHED();
    }
    if (RD) {
      if (j >= 2 && j < 6) na[j - 2] = lds_b128(vA + (j - 2) * 2048);
      else if (j >= 6 && j < 6 + 2 * NB) nw[j - 6] = lds_b128(vB + (j - 6) * 2048);
      else if (j >= 6 + 2 * NB && j < 6 + NT) {
        const unsigned q = aF[j - 6 - 2 * NB] + fo;
        nw[j - 6] = cat8(lds_tr(q), lds_tr(q + 512));
      }
    }
    if (PRE && j == 4 * NT - 5) fl = *(volatile LAS u32x4*)(size_t)M_FULL;
    SCHED();
  }
}

template <int NB, int NF>
__device__ __forceinline__ void p1_mfma(const Params& P, Geo& G, rsrc_t rs, int wid, int lane, int pair0, int offF, int strB, int strF,
                                        int fstep0, int hstep0, int csf0) {
  constexpr int NT = 2 * NB + 2 * NF;
  const int g = lane >> 4, i = lane & 15, wm = wid >> 1;
  f32x4 acc[4][NT];
#pragma unroll
  for (int m = 0; m < 4; ++m)
#pragma unroll
    for (int n = 0; n < NT; ++n) acc[m][n] = (f32x4){0.f, 0.f, 0.f, 0.f};
  const int sw = (g ^ (i & 7)) * 16;
  const unsigned aA = wm * 8192 + i * 128 + sw, aB = 65536 + pair0 * 4096 + i * 128 + sw;
  const int sg = ((i >> 3) & 1) | ((g & 1) << 1);
  unsigned aF[4];
#pragma unroll
  for (int n = 0; n < 4; ++n) aF[n] = offF + (8 * g + (i >> 2)) * 128 + ((n ^ sg) * 32) + 8 * (i & 3);
  const unsigned vprog = M_PROG + wid * 4;
  bf16x8 a0[4], w0[NT], a1[4], w1[NT];
  TS(1);
  spin_ge4(M_FULL, 1u);
  TS(10); TC(58);
#pragma unroll
  for (int m = 0; m < 4; ++m) a0[m] = lds_b128(aA + m * 2048);
#pragma unroll
  for (int n = 0; n < 2 * NB; ++n) w0[n] = lds_b128(aB + n * 2048);
#pragma unroll
  for (int n = 0; n < 2 * NF; ++n) w0[2 * NB + n] = cat8(lds_tr(aF[n]), lds_tr(aF[n] + 512));
  u32x4 fl = {0u, 0u, 0u, 0u};
  int fs = 1;
#define NEXT_FS() do { fs = (fs == 6) ? 0 : fs + 1; } while (0)
#pragma unroll 1
  for (int s = 0; s < 9; ++s) {
    p1_half<NB, NF, true, false, true>(acc, a0, w0, a1, w1, (aA + (s & 3) * 16384) ^ 64, (aB + (s & 3) * strB) ^ 64, aF, fs * strF, fl, 0, vprog, 2 * s + 1);
    NEXT_FS();
    p1_half<NB, NF, true, true, false>(acc, a1, w1, a0, w0, aA + ((s + 1) & 3) * 16384, aB + ((s + 1) & 3) * strB, aF, fs * strF, fl, s + 2, vprog, 2 * s + 2);
    NEXT_FS();
  }
  p1_half<NB, NF, true, false, false>(acc, a0, w0, a1, w1, (aA + 16384) ^ 64, (aB + strB) ^ 64, aF, 5 * strF, fl, 0, vprog, 19);
  p1_half<NB, NF, false, false, false>(acc, a1, w1, a0, w0, 0, 0, aF, 0, fl, 0, vprog, 20);
#undef NEXT_FS
  TS(2); TC(59);
  spin_ge(M_SVCA, 1u);
  spin_ge(M_SVCB, 3u);
  TS(11);
  float mb[4], rd[4];
  {
    const f32x4 s0 = lds_f4(M_STAT + (64 * wm + 4 * i) * 8), s1 = lds_f4(M_STAT + (64 * wm + 4 * i) * 8 + 16);
    mb[0] = s0[0]; mb[1] = s0[2]; mb[2] = s1[0]; mb[3] = s1[2];
    rd[0] = s0[1]; rd[1] = s0[3]; rd[2] = s1[1]; rd[3] = s1[3];
  }
  const unsigned tag = lds_ld(M_TAG);
#define TABLE(k, pos) (C4){lds_f4(M_TAB + (k) * 384 + (pos) * 4), lds_f4(M_TAB + (k) * 384 + 192 + (pos) * 4)}
  C4 Ep[NB];
#pragma unroll
  for (int p = 0; p < NB; ++p) {
    const int pos = 16 * (pair0 + p) + 4 * g;
    const f32x4 csr = lds_f4(M_CS + pos * 4), csi = lds_f4(M_CS + 192 + pos * 4);
    const C4 L1 = TABLE(0, pos);
    C4 h;
#pragma unroll
    for (int m = 0; m < 4; ++m) {
      C4 bu;
      bu.re = fma4(csr, (f32x4)(-mb[m]), acc[m][2 * p]) * rd[m];
      bu.im = fma4(csi, (f32x4)(-mb[m]), acc[m][2 * p + 1]) * rd[m];
      h = (m == 0) ? bu : cfma4(L1, h, bu);
      acc[m][2 * p] = h.re; acc[m][2 * p + 1] = h.im;
    }
    C4 e = h;
    e = cfma4(TABLE(1, pos), row_shr4<1>(e), e);
    e = cfma4(TABLE(2, pos), row_shr4<2>(e), e);
    e = cfma4(TABLE(3, pos), row_shr4<4>(e), e);
    e = cfma4(TABLE(4, pos), row_shr4<8>(e), e);
    if (i == 15) {
      *(LAS f32x4*)(size_t)(M_ENDW + wm * 384 + pos * 4) = e.re;
      *(LAS f32x4*)(size_t)(M_ENDW + wm * 384 + 192 + pos * 4) = e.im;
    }
    Ep[p] = row_shr4<1>(e);
  }
  WAIT_L(0);
  lds_inc(M_CNTE);
  TS(12);
  const int afv = W_AF + (G.c * 8 + 4 * wm) * 90112 + lane * 16;
  const u32x2 flg = {tag, ~tag};
  if (NF > 0) {
#pragma unroll
    for (int f = 0; f < NF; ++f) {
      const f32x4 cs0 = lds_f4(M_CS + (csf0 + 32 * f + 4 * g) * 4), cs1 = lds_f4(M_CS + (csf0 + 32 * f + 16 + 4 * g) * 4);
#pragma unroll
      for (int m = 0; m < 4; ++m) {
        const u32x2 o0 = silu_pack4(fma4(cs0, (f32x4)(-mb[m]), acc[m][2 * NB + 2 * f]) * rd[m]);
        const u32x2 o1 = silu_pack4(fma4(cs1, (f32x4)(-mb[m]), acc[m][2 * NB + 2 * f + 1]) * rd[m]);
        st_sc1((u32x4){o0[0], o0[1], o1[0], o1[1]}, rs, afv + m * 90112 + f * 1024, fstep0 * 1024);
      }
    }
  }
  TS(13);
  C4 PW[NB], CL[NB][4];
#pragma unroll
  for (int p = 0; p < NB; ++p) {
    const int pos = 16 * (pair0 + p) + 4 * g;
    const C4 CF = TABLE(5, pos);
    PW[p] = (C4){lds_f4(M_PW + i * 400 + pos * 4), lds_f4(M_PW + i * 400 + 192 + pos * 4)};
#pragma unroll
    for (int m = 0; m < 4; ++m) {
      CL[p][m] = TABLE(6 + m, pos);
      const C4 l = cmul4(CF, (C4){acc[m][2 * p], acc[m][2 * p + 1]});
      acc[m][2 * p] = l.re; acc[m][2 * p + 1] = l.im;
    }
  }
  TS(14);
  WAIT_V(0);
  if (lane == 0) __builtin_amdgcn_raw_buffer_store_b64(flg, rs, W_FLAGA + ((G.c * 16 + G.pid) * 4 + wid) * 8, 0, 16);
  TS(3);
#undef TABLE
  spin_ge(M_CARRY, 1u);
  TS(4);
#pragma unroll
  for (int p = 0; p < NB; ++p) {
    const int pos = 16 * (pair0 + p) + 4 * g;
    const C4 S = {lds_f4(M_SIN + wm * 384 + pos * 4), lds_f4(M_SIN + wm * 384 + 192 + pos * 4)};
    const C4 X = cfma4(PW[p], S, Ep[p]);
#pragma unroll
    for (int m = 0; m < 4; ++m) {
      const C4 h = cfma4(CL[p][m], X, (C4){acc[m][2 * p], acc[m][2 * p + 1]});
      const u32x2 x0 = __builtin_amdgcn_permlane32_swap(pack2(h.re[0], h.re[1]), pack2(-h.im[0], -h.im[1]), false, false);
      const u32x2 x1 = __builtin_amdgcn_permlane32_swap(pack2(h.re[2], h.re[3]), pack2(-h.im[2], -h.im[3]), false, false);
      st_sc1((u32x4){x0[0], x1[0], x0[1], x1[1]}, rs, afv + m * 90112 + p * 1024, hstep0 * 1024);
    }
  }
  TS(5);
}

template <int ROLE>
__device__ __forceinline__ void p2_mfma(const Params& P, Geo& G, rsrc_t rs, int wid, int lane, int ct, int q0) {
  constexpr int NS = ROLE ? 40 : 48, NQ = NS / 4, DP = ROLE ? 14 : 16;
  const int g = lane >> 4, i = lane & 15;
  const int tl = 64 * (wid >> 1) + 4 * i + 2 * (wid & 1);
  const GAS char* xrow = (const GAS char*)P.x + (size_t)(128 * G.c + tl) * 1280 + (80 * ct + 4 * g) * 2;
  f32x4 acc[2][5];
  const unsigned tag = lds_ld(M_TAG);
  const int vflag = W_FLAGH + ((G.c * 16 + G.pid) * 4 + wid) * 8;
#define PUB_H() do { if (lane == 0) __builtin_amdgcn_raw_buffer_store_b64((u32x2){tag, ~tag}, rs, vflag, 0, 16); } while (0)
  bool early = false;
  if (ROLE) {
    u32x2 dv[5], xv[5][2];
#pragma unroll
    for (int n = 0; n < 5; ++n) {
      dv[n] = *(const GAS u32x2*)((const GAS char*)P.dskip + (80 * ct + 16 * n + 4 * g) * 2);
#pragma unroll
      for (int m = 0; m < 2; ++m) xv[n][m] = *(const GAS u32x2*)(xrow + m * 1280 + n * 32);
    }
    WAIT_V(15);
    PUB_H();
    TS(16);
    const f32x4 st = lds_f4(M_STAT + tl * 8);
#pragma unroll
    for (int n = 0; n < 5; ++n) {
      const f32x4 df = {lo_f(dv[n][0]), hi_f(dv[n][0]), lo_f(dv[n][1]), hi_f(dv[n][1])};
#pragma unroll
      for (int m = 0; m < 2; ++m) {
        const f32x4 xf = {lo_f(xv[n][m][0]), hi_f(xv[n][m][0]), lo_f(xv[n][m][1]), hi_f(xv[n][m][1])};
#pragma unroll
        for (int e = 0; e < 4; ++e) acc[m][n][e] = 0.5f * bfr(bfr(bfr(xf[e] - st[2 * m]) * st[2 * m + 1]) * df[e]);
        asm volatile("" : "+v"(acc[m][n]));
      }
    }
  } else {
    early = __builtin_amdgcn_readfirstlane(lds_ld(M_GO)) != 0u;
    if (!early) { WAIT_V(0); PUB_H(); TS(16); }
#pragma unroll
    for (int m = 0; m < 2; ++m)
#pragma unroll
      for (int n = 0; n < 5; ++n) acc[m][n] = (f32x4){0.f, 0.f, 0.f, 0.f};
  }
  unsigned bW[4];
  if (ROLE) {
#pragma unroll
    for (int k = 0; k < 4; ++k) bW[k] = (g >> 1) * 10240 + i * 128 + (((2 * k + (g & 1)) ^ (i & 7)) * 16);
  } else bW[0] = (4 * g + (i >> 2)) * 160 + 8 * (i & 3);
  const int v0 = W_AF + (G.c * 8 + 2 * wid) * 90112 + (ROLE ? 48 * 1024 : 0) + lane * 16, v1 = v0 + 90112;
  const int gv = W_GB + ((G.c * 4 + wid) * 40 + 5 * ct) * 2048 + lane * 16;
  const unsigned vprog = M_PROG + wid * 4;
  u32x4 ra[DP][2], gq[5][2];
  u32x2 xq[5][2];
  bf16x8 wf[2][5];
  unsigned fl = 0, vb = 0;
#define P2_SOFF(s) ((((((s) >> 2) + q0) >= NQ) ? ((s) >> 2) + q0 - NQ : ((s) >> 2) + q0) * 4096)
#define P2_BASE(s) opaque(ROLE ? bW[(s) & 3] + (((s) >> 2) % 7) * SLOT2 : bW[0] + (((s) >> 2) % 7) * SLOT2)
#define P2_READ(s, n) (ROLE ? lds_b128(vb + (n) * 2048) : cat8(lds_tr(vb + ((s) & 3) * 5120 + (n) * 32), lds_tr(vb + ((s) & 3) * 5120 + 2560 + (n) * 32)))
  spin_ge(M_GO, 1u);
  TS(6);
#pragma unroll
  for (int s = 0; s < DP - 1; ++s) { ra[s][0] = ld_sc1(rs, v0 + (s & 3) * 1024, P2_SOFF(s)); ra[s][1] = ld_sc1(rs, v1 + (s & 3) * 1024, P2_SOFF(s)); }
  SCHED();
  if (!ROLE && early) { WAIT_V(2 * (DP - 1)); PUB_H(); TS(16); }
  spin_ge(M_FULL, 11u);
  vb = P2_BASE(0);
#pragma unroll
  for (int n = 0; n < 5; ++n) wf[0][n] = P2_READ(0, n);
  SCHED();
#pragma unroll
  for (int s = 0; s < NS; ++s) {
    const int r0 = ((s & 3) == 3) ? 2 : 1;
#pragma unroll
    for (int j = 0; j < 10; ++j) {
      acc[j & 1][j >> 1] = __builtin_amdgcn_mfma_f32_16x16x32_bf16(wf[s & 1][j >> 1], __builtin_bit_cast(bf16x8, ra[s % DP][j & 1]), acc[j & 1][j >> 1], 0, 0, 0);
      SCHED();
      if (j == 0 && s + 1 < NS && (ROLE || (s & 3) == 3)) vb = P2_BASE(s + 1);
      if ((s & 3) == 3 && j == 1) {
        WAIT_L(0);
        lds_st1(vprog, 21 + (s >> 2));
        TS(40 + (s >> 2));
        if (s + 1 < NS) { if (__builtin_amdgcn_readfirstlane(fl) < 12u + (s >> 2)) spin_ge(M_FULL + (((s >> 2) + 1) & 1) * 4, 12u + (s >> 2)); }
      }
      if (s + 1 < NS && j >= r0 && j < r0 + 5) wf[(s + 1) & 1][j - r0] = P2_READ(s + 1, j - r0);
      if ((s & 3) == 2 && j == 6 && s + 2 < NS) fl = lds_ld(M_FULL + (((s >> 2) + 1) & 1) * 4);
      if (j == 7 || j == 8) {
        const int sl = s - 1 + DP;
        if (sl < NS) ra[sl % DP][j - 7] = ld_sc1(rs, (j == 7 ? v0 : v1) + (sl & 3) * 1024, P2_SOFF(sl));
        else if (ROLE && s >= 28 && s < 33) xq[s - 28][j - 7] = *(const GAS u32x2*)(xrow + (j - 7) * 1280 + (s - 28) * 32);
        else if (ROLE && s >= 35) gq[s - 35][j - 7] = ld_sc1(rs, gv + (s - 35) * 2048 + (j - 7) * 1024, 0);
      }
      SCHED();
    }
  }
#undef P2_SOFF
#undef P2_BASE
#undef P2_READ
  TS(7);
  if (ROLE == 0) {
#pragma unroll
    for (int n = 0; n < 5; ++n)
#pragma unroll
      for (int m = 0; m < 2; ++m) {
        const u32x2 s = silu_pack4(acc[m][n]);
        st_sc1((u32x4){s[0], tag, s[1], tag}, rs, gv + n * 2048 + m * 1024, 0);
      }
  } else {
#pragma clang loop unroll(disable)
    for (unsigned sp = 0; sp < SPIN_CAP; ++sp) {
      bool ok = true;
#pragma unroll
      for (int n = 0; n < 5; ++n)
#pragma unroll
        for (int m = 0; m < 2; ++m) ok &= (gq[n][m][1] == tag) & (gq[n][m][3] == tag);
      if (__all(ok)) break;
      __builtin_amdgcn_s_sleep(2);
      MEMBAR();
#pragma unroll
      for (int n = 0; n < 5; ++n) { gq[n][0] = ld_sc1(rs, gv + n * 2048, 0); gq[n][1] = ld_sc1(rs, gv + n * 2048 + 1024, 0); }
    }
    GAS char* orow = (GAS char*)P.out + (size_t)(128 * G.c + tl) * 2560 + (80 * ct + 4 * g) * 4;
#pragma unroll
    for (int n = 0; n < 5; ++n)
#pragma unroll
      for (int m = 0; m < 2; ++m) {
        const f32x4 xf = {lo_f(xq[n][m][0]), hi_f(xq[n][m][0]), lo_f(xq[n][m][1]), hi_f(xq[n][m][1])};
        const f32x4 gf = {lo_f(gq[n][m][0]), hi_f(gq[n][m][0]), lo_f(gq[n][m][2]), hi_f(gq[n][m][2])};
        *(GAS f32x4*)(orow + m * 2560 + n * 64) = xf + (acc[m][n] * 2.f) * gf;
      }
  }
  TS(8);
}

template <int TYPE>
__device__ __forceinline__ void loader1(const Params& P, Geo& G, int H, int lane) {
  constexpr int NPB = TYPE ? 2 : 3, NCG = TYPE ? 2 : 1;
  constexpr int STRB = TYPE ? 8192 : 12288, OFFF = TYPE ? 98304 : 114688, STRF = TYPE ? 8192 : 4096;
  constexpr int NG = 4 + NPB + NCG, NS = NG + NCG;
  const int r8 = lane >> 3, c8 = lane & 7, h = H & 1, hh = H >> 1;
  const int chb = TYPE ? 384 + 32 * G.a : 48 * G.a;
  const int colb = TYPE ? 512 + 128 * G.a : 64 * G.a;
  const rsrc_t rX = mk_rsrc(P.x, 2048 * 1280), rB = mk_rsrc(hh ? P.Bim : P.Bre, 640 * 1280), rW = mk_rsrc(P.w1, 640 * 3072);
  const int vA = 4 * r8 * 1280 + ((c8 ^ r8) * 16);
  const int vB = (8 * (r8 >> 2) + (r8 & 3)) * 1280 + ((c8 ^ r8) * 16);
  const int vF = (8 * h + r8) * 3072 + ((((c8 >> 1) ^ (((r8 >> 1) & 1) | (h << 1))) << 1) | (c8 & 1)) * 16;
  const int sA = (G.c * 128 + 32 * h + hh) * 1280, sB = (chb + 4 * h) * 1280, sF = colb * 2 + 16 * hh * 3072;
  const unsigned lb = H * 1024, vfull = M_FULL + H * 4;
  auto issueF = [&](int s, int x) {
    int f = 2 * s + x; f = f >= 14 ? f - 14 : f >= 7 ? f - 7 : f;
#pragma unroll
    for (int k = 0; k < NCG; ++k) dma(rW, vF, sF + (64 * s + 32 * x) * 3072 + k * 128, lb + OFFF + f * STRF + k * 4096);
  };
  auto issueG = [&](int s) {
    const int sl = s & 3;
#pragma unroll
    for (int k = 0; k < 4; ++k) dma(rX, vA, sA + (64 * (k >> 1) + 2 * (k & 1)) * 1280 + 128 * s, lb + sl * 16384 + k * 4096);
#pragma unroll
    for (int k = 0; k < NPB; ++k) dma(rB, vB, sB + 16 * k * 1280 + 128 * s, lb + 65536 + sl * STRB + k * 4096);
    issueF(s, 0);
  };
  issueG(0);
  BAR();
  issueF(0, 1); issueG(1); issueF(1, 1); issueG(2); issueF(2, 1);
  TS(1);
  WAIT_V(2 * NS);
  lds_st1(vfull, 1u);
  TS(10);
  issueG(3);
  WAIT_V(NS + NG);
  lds_st1(vfull, 2u);
  TS(11);
  spin_prog(1);
  issueF(3, 1);
#pragma unroll 1
  for (int s = 4; s < 10; ++s) {
    spin_prog(2 * s - 6);
    TS(20 + s);
    issueG(s);
    TS(30 + s);
    WAIT_V(NS + NG);
    lds_st1(vfull, s - 1);
    TS(40 + s);
    spin_prog(2 * s - 5);
    issueF(s, 1);
  }
}
template <int ROLE>
__device__ __forceinline__ void loader2(const Params& P, Geo& G, int H, int lane, int ct, int q0) {
  constexpr int NQ = ROLE ? 10 : 12;
  const int r8 = lane >> 3, c8 = lane & 7;
  const rsrc_t r0 = ROLE ? mk_rsrc(P.Cre, 640 * 1280) : mk_rsrc(P.w2, 1536 * 1280), r1 = ROLE ? mk_rsrc(P.Cim, 640 * 1280) : r0;
  int vo[5];
  if (ROLE) vo[0] = r8 * 1280 + ((c8 ^ r8) * 16);
  else {
#pragma unroll
    for (int v = 0; v < 5; ++v) { const int t = 64 * v + lane; vo[v] = (t / 10) * 1280 + (t % 10) * 16; }
  }
  auto issue = [&](int q) {
    const int qq = q + q0 >= NQ ? q + q0 - NQ : q + q0;
    const int sl = (q >= 7 ? q - 7 : q) * SLOT2;
#pragma unroll
    for (int j = 0; j < 20; ++j) {
      if (ROLE) dma(j >= 10 ? r1 : r0, vo[0], (80 * ct + 8 * (j % 10)) * 1280 + 128 * qq, sl + j * 1024);
      else dma(r0, vo[j % 5], (128 * qq + 32 * (j / 5)) * 1280 + 160 * ct, sl + j * 1024);
    }
  };
  spin_prog(20);
  TS(12);
  int qi = H, qp = H;
#pragma unroll 1
  while (qp < NQ) {
    if (qi < NQ) {
      const int depth = (__builtin_amdgcn_readfirstlane(lds_ld(M_GO)) != 0u) ? 6 : L2_DEPTH;
      if (qi - qp < depth) {
        if (qi == qp) spin_prog(14 + qi);
        if (qi == qp || read_prog() >= (unsigned)(14 + qi)) { issue(qi); qi += 2; continue; }
      }
    }
    if (qi - qp == 2) WAIT_V(0); else if (qi - qp == 4) WAIT_V(20); else WAIT_V(40);
    lds_st1(M_FULL + H * 4, 11 + qp);
    TS(50 + qp);
    qp += 2;
  }
}

template <int NS>
__device__ __forceinline__ void service_h2(const Params& P, Geo& G, rsrc_t rs, int lane, int role) {
  const int nch = G.type ? 32 : 48, chb = G.type ? 384 + 32 * G.a : 48 * G.a;
  const int ncol = G.type ? 128 : 64, colb = G.type ? 512 + 128 * G.a : 64 * G.a;
  const int vs = W_STAT + (128 * G.c + lane) * 16;
  const int vb = W_CSB + (chb + (lane < nch ? lane : 0)) * 16;
  const int vf = W_CSF + (colb + (2 * lane < ncol ? 2 * lane : 0)) * 8;
  u32x4 s0, s1, cb, cf[10];
  s0 = ld_sc1(rs, vs, 0); s1 = ld_sc1(rs, vs + 1024, 0); cb = ld_sc1(rs, vb, 0);
#pragma unroll
  for (int k = 0; k < 10; ++k) cf[k] = ld_sc1(rs, vf + k * 12288, 0);
  SCHED();
  WAIT_V(NS + 13);
  lds_st1(M_FULL + 8, 9u);
  WAIT_V(13);
  lds_st1(M_FULL + 8, 10u);
  spin_ge(M_SVCB, 2u);
  const unsigned tag = lds_ld(M_TAG);
#pragma clang loop unroll(disable)
  for (unsigned sp = 0; sp < SPIN_CAP; ++sp) {
    bool ok = (s0[1] == tag) & (s0[3] == tag) & (s1[1] == tag) & (s1[3] == tag) & (cb[1] == tag) & (cb[3] == tag);
#pragma unroll
    for (int k = 0; k < 10; ++k) ok &= (cf[k][1] == tag) & (cf[k][3] == tag);
    if (__all(ok)) break;
    __builtin_amdgcn_s_sleep(4);
    MEMBAR();
    s0 = ld_sc1(rs, vs, 0); s1 = ld_sc1(rs, vs + 1024, 0); cb = ld_sc1(rs, vb, 0);
#pragma unroll
    for (int k = 0; k < 10; ++k) cf[k] = ld_sc1(rs, vf + k * 12288, 0);
  }
  *(LAS u32x2*)(size_t)(M_STAT + lane * 8) = (u32x2){s0[0], s0[2]};
  *(LAS u32x2*)(size_t)(M_STAT + 512 + lane * 8) = (u32x2){s1[0], s1[2]};
  if (lane < nch) {
    *(LAS unsigned*)(size_t)(M_CS + chpos(lane) * 4) = cb[0];
    *(LAS unsigned*)(size_t)(M_CS + 192 + chpos(lane) * 4) = cb[2];
  }
  if (2 * lane < ncol) {
    float c0 = 0.f, c1 = 0.f;
#pragma unroll
    for (int k = 0; k < 10; ++k) { c0 += __uint_as_float(cf[k][0]); c1 += __uint_as_float(cf[k][2]); }
    *(LAS f32x2*)(size_t)(M_CS + (96 + 2 * lane) * 4) = (f32x2){c0, c1};
  }
  WAIT_L(0);
  lds_st1(M_SVCA, 1u);
  TS(2);
  const int vfl = (role ? W_FLAGH : W_FLAGA) + G.c * 512 + lane * 8;
  spin_ge(role ? M_CARRY : M_CNTE, role ? 1u : 4u);
#pragma clang loop unroll(disable)
  for (unsigned sp = 0; sp < SPIN_CAP; ++sp) {
    const u32x2 f = __builtin_amdgcn_raw_buffer_load_b64(rs, vfl, 0, 16);
    if (__all((f[0] == tag) & (f[1] == ~tag))) break;
    __builtin_amdgcn_s_sleep(2);
    MEMBAR();
  }
  MEMBAR();
  lds_st1(M_GO, 1u);
  TS(3);
}
__device__ __forceinline__ void service_h3(const Params& P, Geo& G, rsrc_t rs, int lane) {
  const int nch = G.type ? 32 : 48, chb = G.type ? 384 + 32 * G.a : 48 * G.a;
  const int cl = lane < nch ? lane : 0, pos = chpos(cl);
  spin_ge(M_SVCB, 2u);
  const unsigned tag = lds_ld(M_TAG);
  const f32x2 L4 = {*(const LAS float*)(size_t)(M_TAB + 384 + pos * 4), *(const LAS float*)(size_t)(M_TAB + 576 + pos * 4)};
  const f32x2 L32 = {*(const LAS float*)(size_t)(M_TAB + 4 * 384 + pos * 4), *(const LAS float*)(size_t)(M_TAB + 4 * 384 + 192 + pos * 4)};
  const f32x2 L64 = cmul(L32, L32), L128 = cmul(L64, L64);
  spin_prog(14);
  if (lane < nch) {
    f32x2 pw = {1.f, 0.f};
#pragma unroll
    for (int i = 0; i < 16; ++i) {
      *(LAS float*)(size_t)(M_PW + i * 400 + pos * 4) = pw.x;
      *(LAS float*)(size_t)(M_PW + i * 400 + 192 + pos * 4) = pw.y;
      pw = cmul(pw, L4);
    }
  }
  WAIT_L(0);
  lds_inc(M_SVCB);
  TS(2);
  spin_ge(M_CNTE, 4u);
  TS(4);
  const f32x2 e0 = {*(const LAS float*)(size_t)(M_ENDW + pos * 4), *(const LAS float*)(size_t)(M_ENDW + 192 + pos * 4)};
  const f32x2 e1 = {*(const LAS float*)(size_t)(M_ENDW + 384 + pos * 4), *(const LAS float*)(size_t)(M_ENDW + 576 + pos * 4)};
  const int ve = W_EGR + (chb + cl) * 16;
  if (G.c < 15 && lane < nch) {
    const f32x2 ec = cfma(L64, e0, e1);
    st_sc1((u32x4){__float_as_uint(ec.x), tag, __float_as_uint(ec.y), tag}, rs, ve + G.c * 10240, 0);
  }
  f32x2 S = {0.f, 0.f};
  if (G.c > 0) {
    u32x4 ev[15];
#pragma clang loop unroll(disable)
    for (unsigned sp = 0; sp < SPIN_CAP; ++sp) {
      bool ok = true;
#pragma unroll
      for (int j = 0; j < 15; ++j) ev[j] = ld_sc1(rs, ve + (j < G.c ? j : 0) * 10240, 0);
#pragma unroll
      for (int j = 0; j < 15; ++j) ok &= (ev[j][1] == tag) & (ev[j][3] == tag);
      if (__all(ok)) break;
      __builtin_amdgcn_s_sleep(1);
      MEMBAR();
    }
#pragma unroll
    for (int j = 0; j < 15; ++j)
      if (j < G.c) S = cfma(L128, S, (f32x2){__uint_as_float(ev[j][0]), __uint_as_float(ev[j][2])});
  }
  if (lane < nch) {
    const f32x2 S1 = cfma(L64, S, e0);
    *(LAS float*)(size_t)(M_SIN + pos * 4) = S.x; *(LAS float*)(size_t)(M_SIN + 192 + pos * 4) = S.y;
    *(LAS float*)(size_t)(M_SIN + 384 + pos * 4) = S1.x; *(LAS float*)(size_t)(M_SIN + 576 + pos * 4) = S1.y;
  }
  WAIT_L(0);
  lds_st1(M_CARRY, 1u);
  TS(3);
}
template <int TYPE>
__device__ __forceinline__ void helper1(const Params& P, Geo& G, rsrc_t rs, int H, int lane, int role) {
  constexpr int NS = TYPE ? 10 : 9;
  loader1<TYPE>(P, G, H, lane);
  if (H == 2) service_h2<NS>(P, G, rs, lane, role);
  else {
    WAIT_V(NS);
    lds_st1(M_FULL + H * 4, 9u);
    WAIT_V(0);
    lds_st1(M_FULL + H * 4, 10u);
  }
}

__global__ __launch_bounds__(512, 2) void fused_kernel(const Params P) {
  __shared__ __attribute__((aligned(1024))) char lds_[163840];
  asm volatile("" ::"v"((LAS char*)lds_) : "memory");
  const int tid = threadIdx.x, wid = __builtin_amdgcn_readfirstlane(tid >> 6), lane = tid & 63;
  const int b = blockIdx.x, xcd = b & 7, r = b >> 3;
  Geo G;
  G.c = 4 * (xcd >> 1) + (r & 3); G.jj = r >> 2; G.type = G.jj & 1; G.par = xcd & 1;
  G.a = 4 * G.par + (G.jj >> 1); G.pid = 8 * G.par + G.jj; G.u = (xcd >> 1) * 32 + r;
  const int role = G.jj >> 2, ct = 4 * G.par + (G.jj & 3);
  const int q0 = role ? ((G.jj & 3) * 10) >> 2 : 3 * (G.jj & 3);
  const rsrc_t rs = mk_rsrc(P.ws, W_END);
  TS(0); TC(62);
  if (wid < 4) {
    if (wid == 0) svc_rowstat(P, G, rs, lane);
    else if (wid == 1) svc_colsum(P, G, rs, lane);
    else if (wid == 2) svc_tables<0>(P, G, rs, lane);
    else svc_tables<1>(P, G, rs, lane);
    const int wn = wid & 1;
    if (G.type == 0 && wn == 0) p1_mfma<2, 0>(P, G, rs, wid, lane, 0, 114688, 12288, 4096, 0, 48 + 3 * G.a, 96);
    else {
      const int pair0 = G.type ? wn : 2;
      const int offF = G.type ? 98304 + wn * 4096 : 114688;
      const int fstep0 = G.type ? 16 + 4 * G.a + 2 * wn : 2 * G.a;
      const int hstep0 = G.type ? 72 + 2 * G.a + wn : 48 + 3 * G.a + 2;
      const int csf0 = G.type ? 96 + 64 * wn : 96;
      p1_mfma<1, 2>(P, G, rs, wid, lane, pair0, offF, G.type ? 8192 : 12288, G.type ? 8192 : 4096, fstep0, hstep0, csf0);
    }
    if (role) p2_mfma<1>(P, G, rs, wid, lane, ct, q0);
    else p2_mfma<0>(P, G, rs, wid, lane, ct, q0);
  } else {
    const int H = wid - 4;
    if (G.type) helper1<1>(P, G, rs, H, lane, role); else helper1<0>(P, G, rs, H, lane, role);
    if (H == 3) service_h3(P, G, rs, lane);
    if (H < 2) {
      if (role) loader2<1>(P, G, H, lane, ct, q0); else loader2<0>(P, G, H, lane, ct, q0);
    }
    if (b == 0 && H == 0) {
      const unsigned tag = lds_ld(M_TAG);
#pragma clang loop unroll(disable)
      for (unsigned sp = 0; sp < SPIN_CAP; ++sp) {
        bool ok = true;
#pragma unroll
        for (int k = 0; k < 4; ++k) {
          const u32x2 f = __builtin_amdgcn_raw_buffer_load_b64(rs, W_FLAGH + (lane * 4 + k) * 32, 0, 16);
          ok &= (f[0] == tag) & (f[1] == ~tag);
        }
        if (__all(ok)) break;
        __builtin_amdgcn_s_sleep(2);
        MEMBAR();
      }
      if (lane == 0) __builtin_amdgcn_raw_buffer_store_b32(tag, rs, W_EPOCH, 0, 16);
    }
  }
  TS(9); TC(63);
}

extern "C" void kernel_launch(void* const* d_in, const int* in_sizes, int n_in, void* d_out, int out_size, void* d_ws,
                              size_t ws_size, hipStream_t stream) {
  Params P;
  P.x = (const char*)d_in[0]; P.lre = (const char*)d_in[1]; P.lim = (const char*)d_in[2];
  P.Bre = (const char*)d_in[3]; P.Bim = (const char*)d_in[4]; P.Cre = (const char*)d_in[5]; P.Cim = (const char*)d_in[6];
  P.logdt = (const char*)d_in[7]; P.dskip = (const char*)d_in[8]; P.w1 = (const char*)d_in[10]; P.w2 = (const char*)d_in[11];
  P.ws = (char*)d_ws; P.out = (float*)d_out;
  fused_kernel<<<256, 512, 0, stream>>>(P);
}
```

```cpp
#include <hip/hip_runtime.h>
#include <hip/hip_bf16.h>
#include <stdint.h>

typedef __attribute__((ext_vector_type(8))) short bf16x8;
typedef __attribute__((ext_vector_type(4))) short s16x4;
typedef __attribute__((ext_vector_type(4))) float f32x4;
typedef __attribute__((ext_vector_type(2))) float f32x2;
typedef __attribute__((ext_vector_type(4))) unsigned u32x4;
typedef __attribute__((ext_vector_type(2))) unsigned u32x2;
typedef __attribute__((ext_vector_type(2))) __bf16 bf2_t;
#define GAS __attribute__((address_space(1)))
#define LAS __attribute__((address_space(3)))
typedef LAS char* lptr;

#define WAIT_V(n) asm volatile("s_waitcnt vmcnt(%0)" ::"n"(n) : "memory")
#define WAIT_L(n) asm volatile("s_waitcnt lgkmcnt(%0)" ::"n"(n) : "memory")
#define SCHED() __builtin_amdgcn_sched_barrier(0)
#define MEMBAR() asm volatile("" ::: "memory")
#define BAR() do { MEMBAR(); __builtin_amdgcn_s_barrier(); MEMBAR(); } while (0)

constexpr int W_EPOCH = 0;
constexpr int W_FLAGA = 4096;
constexpr int W_FLAGH = 12288;
constexpr int W_STAT  = 20480;
constexpr int W_CSB   = 53248;
constexpr int W_CSF   = 65536;
constexpr int W_EGR   = 196608;
constexpr int W_AF    = 393216;
constexpr int W_GB    = W_AF + 16 * 8 * 88 * 1024;
constexpr int W_END   = W_GB + 16 * 4 * 40 * 2048;
constexpr int W_DBG   = W_END;
constexpr int MISC   = 155648;
constexpr int M_FULL = MISC;
constexpr int M_PROG = MISC + 16;
constexpr int M_SVCA = MISC + 32, M_SVCB = MISC + 36, M_CNTE = MISC + 40, M_CARRY = MISC + 44, M_GO = MISC + 48, M_TAG = MISC + 52, M_EGS = MISC + 56;
constexpr int M_STAT = MISC + 256;
constexpr int M_CS   = MISC + 1280;
constexpr int M_TAB  = MISC + 2304;
constexpr int M_ENDW = MISC + 6144;
constexpr int M_SIN  = MISC + 6912;
constexpr int M_DUMMY = MISC + 7680;
constexpr int M_PW   = 147456;
constexpr int SLOT2  = 20480;
constexpr unsigned SPIN_CAP = 1u << 16;
#define L2_DEPTH 2

__device__ __forceinline__ float bf2f(unsigned short v) { return __uint_as_float((unsigned)v << 16); }
__device__ __forceinline__ float bfr(float a) { return (float)(__bf16)a; }
__device__ __forceinline__ unsigned pack2(float a, float b) {
  bf2_t v = {(__bf16)a, (__bf16)b};
  return __builtin_bit_cast(unsigned, v);
}
__device__ __forceinline__ float lo_f(unsigned u) { return __uint_as_float(u << 16); }
__device__ __forceinline__ float hi_f(unsigned u) { return __uint_as_float(u & 0xFFFF0000u); }
__device__ __forceinline__ f32x2 cmul(f32x2 a, f32x2 b) {
  f32x2 r; r.x = a.x * b.x - a.y * b.y; r.y = a.x * b.y + a.y * b.x; return r;
}
__device__ __forceinline__ f32x2 cfma(f32x2 a, f32x2 b, f32x2 c) {
  f32x2 r; r.x = fmaf(a.x, b.x, fmaf(-a.y, b.y, c.x)); r.y = fmaf(a.x, b.y, fmaf(a.y, b.x, c.y)); return r;
}
struct C4 { f32x4 re, im; };
__device__ __forceinline__ f32x4 fma4(f32x4 a, f32x4 b, f32x4 c) { return __builtin_elementwise_fma(a, b, c); }
__device__ __forceinline__ C4 cfma4(const C4& a, const C4& b, const C4& c) {
  C4 r; r.re = fma4(a.re, b.re, fma4(-a.im, b.im, c.re)); r.im = fma4(a.re, b.im, fma4(a.im, b.re, c.im)); return r;
}
__device__ __forceinline__ C4 cmul4(const C4& a, const C4& b) {
  C4 r; r.re = fma4(a.re, b.re, -(a.im * b.im)); r.im = fma4(a.re, b.im, a.im * b.re); return r;
}
template <int N> __device__ __forceinline__ float row_shr(float v) {
  return __builtin_bit_cast(float, __builtin_amdgcn_update_dpp(0, __builtin_bit_cast(int, v), 0x110 + N, 0xf, 0xf, true));
}
template <int N> __device__ __forceinline__ C4 row_shr4(const C4& v) {
  C4 r;
#pragma unroll
  for (int e = 0; e < 4; ++e) { r.re[e] = row_shr<N>(v.re[e]); r.im[e] = row_shr<N>(v.im[e]); }
  return r;
}
__device__ __forceinline__ u32x2 silu_pack4(f32x4 v) {
  const unsigned p0 = pack2(v[0], v[1]), p1 = pack2(v[2], v[3]);
  const f32x4 x = {lo_f(p0), hi_f(p0), lo_f(p1), hi_f(p1)};
  const f32x4 t = x * (-1.44269504088896341f);
  f32x4 d;
#pragma unroll
  for (int e = 0; e < 4; ++e) d[e] = __builtin_amdgcn_exp2f(t[e]);
  d = d + 1.f;
#pragma unroll
  for (int e = 0; e < 4; ++e) d[e] = __builtin_amdgcn_rcpf(d[e]);
  const f32x4 s = x * d;
  return (u32x2){pack2(s[0], s[1]), pack2(s[2], s[3])};
}
__device__ __forceinline__ float sum8(u32x4 v) {
  return ((lo_f(v[0]) + hi_f(v[0])) + (lo_f(v[1]) + hi_f(v[1]))) + ((lo_f(v[2]) + hi_f(v[2])) + (lo_f(v[3]) + hi_f(v[3])));
}
typedef __amdgpu_buffer_rsrc_t rsrc_t;
__device__ __forceinline__ rsrc_t mk_rsrc(const void* p, int bytes) { return __builtin_amdgcn_make_buffer_rsrc((void*)p, 0, bytes, 0x00020000); }
__device__ __forceinline__ void dma(rsrc_t r, int voff, int soff, unsigned l) {
  __builtin_amdgcn_raw_ptr_buffer_load_lds(r, (LAS void*)(size_t)l, 16, voff, soff, 0, 0);
}
__device__ __forceinline__ void touch(rsrc_t r, int voff, int soff) {
  __builtin_amdgcn_raw_ptr_buffer_load_lds(r, (LAS void*)(size_t)M_DUMMY, 4, voff, soff, 0, 0);
}
__device__ __forceinline__ void touch_sc1(rsrc_t r, int voff) {
  __builtin_amdgcn_raw_ptr_buffer_load_lds(r, (LAS void*)(size_t)M_DUMMY, 4, voff, 0, 0, 16);
}
__device__ __forceinline__ bf16x8 lds_b128(unsigned a) { return *(const LAS bf16x8*)(size_t)a; }
__device__ __forceinline__ f32x4 lds_f4(unsigned a) { return *(const LAS f32x4*)(size_t)a; }
__device__ __forceinline__ s16x4 lds_tr(unsigned a) { return __builtin_amdgcn_ds_read_tr16_b64_v4i16((LAS s16x4*)(size_t)a); }
__device__ __forceinline__ bf16x8 cat8(s16x4 a, s16x4 b) { return __builtin_shufflevector(a, b, 0, 1, 2, 3, 4, 5, 6, 7); }
__device__ __forceinline__ unsigned opaque(unsigned v) { asm volatile("" : "+v"(v)); return v; }
__device__ __forceinline__ u32x4 ld_sc1(rsrc_t r, int voff, int soff) { return __builtin_amdgcn_raw_buffer_load_b128(r, voff, soff, 16); }
__device__ __forceinline__ void st_sc1(u32x4 v, rsrc_t r, int voff, int soff) { __builtin_amdgcn_raw_buffer_store_b128(v, r, voff + soff, 0, 16); }
__device__ __forceinline__ unsigned lds_ld(unsigned a) { return *(volatile LAS unsigned*)(size_t)a; }
__device__ __forceinline__ void lds_st1(unsigned a, unsigned v) {
  asm volatile("s_mov_b64 exec, 1\n\tds_write_b32 %0, %1\n\ts_mov_b64 exec, -1" ::"v"(a), "v"(v) : "memory");
}
__device__ __forceinline__ void spin_ge(unsigned a, unsigned need) {
#pragma clang loop unroll(disable)
  for (unsigned s = 0; s < (1u << 22); ++s) {
    if (__builtin_amdgcn_readfirstlane(lds_ld(a)) >= need) break;
    __builtin_amdgcn_s_sleep(1);
  }
  MEMBAR();
}
__device__ __forceinline__ unsigned min4(u32x4 p) {
  const unsigned a = p[0] < p[1] ? p[0] : p[1], b = p[2] < p[3] ? p[2] : p[3];
  return a < b ? a : b;
}
__device__ __forceinline__ void spin_ge4(unsigned a, unsigned need) {
#pragma clang loop unroll(disable)
  for (unsigned s = 0; s < (1u << 18); ++s) {
    if (__builtin_amdgcn_readfirstlane(min4(*(volatile LAS u32x4*)(size_t)a)) >= need) break;
    __builtin_amdgcn_s_sleep(1);
  }
  MEMBAR();
}
__device__ __forceinline__ float dsum2(unsigned v, float c) {
  const bf2_t one = {(__bf16)1.f, (__bf16)1.f};
  return __builtin_amdgcn_fdot2_f32_bf16(__builtin_bit_cast(bf2_t, v), one, c, false);
}
__device__ __forceinline__ unsigned read_prog() {
  const u32x4 p = *(volatile LAS u32x4*)(size_t)M_PROG;
  const unsigned a = p[0] < p[1] ? p[0] : p[1], b = p[2] < p[3] ? p[2] : p[3];
  return __builtin_amdgcn_readfirstlane(a < b ? a : b);
}
__device__ __forceinline__ void spin_prog(unsigned need) {
#pragma clang loop unroll(disable)
  for (unsigned s = 0; s < (1u << 22); ++s) {
    const u32x4 p = *(volatile LAS u32x4*)(size_t)M_PROG;
    const unsigned a = p[0] < p[1] ? p[0] : p[1], b = p[2] < p[3] ? p[2] : p[3];
    if (__builtin_amdgcn_readfirstlane(a < b ? a : b) >= need) break;
    __builtin_amdgcn_s_sleep(1);
  }
  MEMBAR();
}
__device__ __forceinline__ void lds_inc(unsigned a) {
  if ((threadIdx.x & 63) == 0) __hip_atomic_fetch_add((LAS unsigned*)(size_t)a, 1u, __ATOMIC_RELAXED, __HIP_MEMORY_SCOPE_WORKGROUP);
}
#define TS(k) do {} while (0)
#define TC(k) do {} while (0)

struct Params {
  const char *x, *lre, *lim, *Bre, *Bim, *Cre, *Cim, *logdt, *dskip, *w1, *w2;
  char* ws;
  float* out;
};
struct Geo {
  int c, type, a, pid, par, u, jj;
};

__device__ __forceinline__ unsigned ld_epoch(rsrc_t rs) { return __builtin_amdgcn_raw_buffer_load_b32(rs, W_EPOCH, 0, 16); }
__device__ __forceinline__ int chpos(int lc) {
  const int w = lc & 15;
  return (lc & 48) + 4 * (2 * ((w >> 2) & 1) + (w >> 3)) + (w & 3);
}
__device__ __forceinline__ void svc_rowstat(const Params& P, Geo& G, rsrc_t rs, int lane) {
  const int r8 = lane >> 3, c8 = lane & 7;
  const unsigned ep = ld_epoch(rs);
  u32x4 v[10];
  const GAS char* src = (const GAS char*)P.x + (size_t)(128 * G.c + 8 * G.pid + r8) * 1280 + c8 * 16;
#pragma unroll
  for (int q = 0; q < 10; ++q) v[q] = *(const GAS u32x4*)(src + q * 128);
  if (lane < 16) *(LAS unsigned*)(size_t)(MISC + lane * 4) = 0u;
  WAIT_L(0);
  BAR();
  float s0 = 0.f, s1 = 0.f;
#pragma unroll
  for (int q = 0; q < 10; ++q) { s0 = dsum2(v[q][0], s0); s1 = dsum2(v[q][1], s1); s0 = dsum2(v[q][2], s0); s1 = dsum2(v[q][3], s1); }
  float s = s0 + s1;
  s += __shfl_xor(s, 1); s += __shfl_xor(s, 2); s += __shfl_xor(s, 4);
  const float mean = s * (1.f / 640.f);
  f32x2 q0 = {0.f, 0.f}, q1 = {0.f, 0.f};
#pragma unroll
  for (int q = 0; q < 10; ++q)
#pragma unroll
    for (int e = 0; e < 4; e += 2) {
      const f32x2 a = (f32x2){lo_f(v[q][e]), hi_f(v[q][e])} - mean, b = (f32x2){lo_f(v[q][e + 1]), hi_f(v[q][e + 1])} - mean;
      q0 = __builtin_elementwise_fma(a, a, q0); q1 = __builtin_elementwise_fma(b, b, q1);
    }
  float ss = (q0[0] + q0[1]) + (q1[0] + q1[1]);
  ss += __shfl_xor(ss, 1); ss += __shfl_xor(ss, 2); ss += __shfl_xor(ss, 4);
  const float mb = bfr(mean);
  const float den = bfr(sqrtf(bfr(bfr(ss * (1.f / 640.f)) + 1e-5f)));
  if (c8 == 0) {
    const unsigned tag = ep + 1u;
    st_sc1((u32x4){__float_as_uint(mb), tag, __float_as_uint(1.f / den), tag}, rs, W_STAT + (128 * G.c + 8 * G.pid + r8) * 16, 0);
  }
}
__device__ __forceinline__ void svc_colsum(const Params& P, Geo& G, rsrc_t rs, int lane) {
  const int r8 = lane >> 3, c8 = lane & 7;
  const unsigned ep = ld_epoch(rs);
  const int jb = G.u + 128 * (r8 >> 1);
  const bool bok = (r8 < 6) && (jb < 320);
  const int jc = bok ? jb : 0;
  const int chs = (jc < 192) ? jc + 192 * G.par : 384 + (jc - 192) + 128 * G.par;
  u32x4 v[10], f[8];
  {
    const GAS char* src = (const GAS char*)((r8 & 1) ? P.Bim : P.Bre) + (size_t)chs * 1280 + c8 * 16;
#pragma unroll
    for (int q = 0; q < 10; ++q) v[q] = *(const GAS u32x4*)(src + q * 128);
  }
  const int uu = G.u < 120 ? G.u : 0;
  const int cgi = uu % 12, kr = uu / 12;
  const int cg = (cgi < 4) ? cgi + 4 * G.par : 8 + (cgi - 4) + 8 * G.par;
  {
    const GAS char* src = (const GAS char*)P.w1 + (size_t)(64 * kr + r8) * 3072 + cg * 128 + c8 * 16;
#pragma unroll
    for (int q = 0; q < 8; ++q) f[q] = *(const GAS u32x4*)(src + q * 8 * 3072);
  }
  BAR();
  const unsigned tag = ep + 1u;
  float s0 = 0.f, s1 = 0.f;
#pragma unroll
  for (int q = 0; q < 10; ++q) { s0 = dsum2(v[q][0], s0); s1 = dsum2(v[q][1], s1); s0 = dsum2(v[q][2], s0); s1 = dsum2(v[q][3], s1); }
  float s = s0 + s1;
  s += __shfl_xor(s, 1); s += __shfl_xor(s, 2); s += __shfl_xor(s, 4);
  const float si = __shfl_xor(s, 8);
  float cs[8];
#pragma unroll
  for (int e = 0; e < 4; ++e) {
    f32x2 a = {0.f, 0.f};
#pragma unroll
    for (int q = 0; q < 8; ++q) a += (f32x2){lo_f(f[q][e]), hi_f(f[q][e])};
    cs[2 * e] = a[0]; cs[2 * e + 1] = a[1];
  }
#pragma unroll
  for (int e = 0; e < 8; ++e) { cs[e] += __shfl_xor(cs[e], 8); cs[e] += __shfl_xor(cs[e], 16); cs[e] += __shfl_xor(cs[e], 32); }
  const int rs4 = r8 & 3;
  const float o0 = rs4 == 0 ? cs[0] : rs4 == 1 ? cs[2] : rs4 == 2 ? cs[4] : cs[6];
  const float o1 = rs4 == 0 ? cs[1] : rs4 == 1 ? cs[3] : rs4 == 2 ? cs[5] : cs[7];
  if (bok && (lane & 15) == 0) st_sc1((u32x4){__float_as_uint(s), tag, __float_as_uint(si), tag}, rs, W_CSB + chs * 16, 0);
  if (G.u < 120 && r8 < 4) st_sc1((u32x4){__float_as_uint(o0), tag, __float_as_uint(o1), tag}, rs, W_CSF + kr * 12288 + (64 * cg + 8 * c8 + 2 * rs4) * 8, 0);
}
template <int PART>
__device__ __forceinline__ void svc_tables(const Params& P, Geo& G, rsrc_t rs, int lane) {
  const int nch = G.type ? 32 : 48, chb = G.type ? 384 + 32 * G.a : 48 * G.a;
  const int cl = lane < nch ? lane : 0, pos = chpos(cl), chp = chb + cl;
  const unsigned short wl = *(const GAS unsigned short*)((const GAS char*)P.logdt + chp * 2);
  const unsigned short wr = *(const GAS unsigned short*)((const GAS char*)P.lre + chp * 2);
  const unsigned short wi = *(const GAS unsigned short*)((const GAS char*)P.lim + chp * 2);
  unsigned ep = 0;
  if (PART) ep = ld_epoch(rs);
  BAR();
  const float dt = bfr(expf(bf2f(wl)));
  const float ar = bf2f(wr), ai = bf2f(wi);
  const float zr = dt * ar, zi = dt * ai;
  const float ex = expf(zr);
  float sn, cs;
  sincosf(zi, &sn, &cs);
  const f32x2 L = {ex * cs, ex * sn};
  const f32x2 L2 = cmul(L, L), L4 = cmul(L2, L2);
  f32x2 tb[5];
  if (PART == 0) {
    tb[0] = L; tb[1] = L4; tb[2] = cmul(L4, L4); tb[3] = cmul(tb[2], tb[2]); tb[4] = cmul(tb[3], tb[3]);
  } else {
    const float nr = L.x - 1.f, ni = L.y, dd = ar * ar + ai * ai;
    const f32x2 CF = {(nr * ar + ni * ai) / dd, (ni * ar - nr * ai) / dd};
    tb[0] = CF; tb[1] = cmul(CF, L); tb[2] = cmul(CF, L2); tb[3] = cmul(CF, cmul(L2, L)); tb[4] = cmul(CF, L4);
  }
  if (lane < nch) {
#pragma unroll
    for (int k = 0; k < 5; ++k) {
      *(LAS float*)(size_t)(M_TAB + (5 * PART + k) * 384 + pos * 4) = tb[k].x;
      *(LAS float*)(size_t)(M_TAB + (5 * PART + k) * 384 + 192 + pos * 4) = tb[k].y;
    }
  }
  if (PART) lds_st1(M_TAG, ep + 1u);
  WAIT_L(0);
  lds_inc(M_SVCB);
}

template <int NB, int NF, bool RD, bool CHK, bool PRE>
__device__ __forceinline__ void p1_half(f32x4 (&acc)[4][2 * NB + 2 * NF], const bf16x8 (&ca)[4], const bf16x8 (&cw)[2 * NB + 2 * NF],
                                        bf16x8 (&na)[4], bf16x8 (&nw)[2 * NB + 2 * NF], unsigned vA, unsigned vB, const unsigned (&aF)[4],
                                        unsigned fo, u32x4& fl, unsigned need, unsigned vprog, unsigned prog) {
  constexpr int NT = 2 * NB + 2 * NF;
#pragma unroll
  for (int j = 0; j < 4 * NT; ++j) {
    acc[j & 3][j >> 2] = __builtin_amdgcn_mfma_f32_16x16x32_bf16(cw[j >> 2], ca[j & 3], acc[j & 3][j >> 2], 0, 0, 0);
    SCHED();
    if (j == 1) {
      WAIT_L(0);
      unsigned z = 0;
      if (CHK) z = __builtin_amdgcn_readfirstlane(min4(fl));
      SCHED();
      lds_st1(vprog, prog);
      if (CHK) { if (z < need) spin_ge4(M_FULL, need); }
      SCHED();
    }
    if (RD) {
      if (j >= 2 && j < 6) na[j - 2] = lds_b128(vA + (j - 2) * 2048);
      else if (j >= 6 && j < 6 + 2 * NB) nw[j - 6] = lds_b128(vB + (j - 6) * 2048);
      else if (j >= 6 + 2 * NB && j < 6 + NT) {
        const unsigned q = aF[j - 6 - 2 * NB] + fo;
        nw[j - 6] = cat8(lds_tr(q), lds_tr(q + 512));
      }
    }
    if (PRE && j == 4 * NT - 5) fl = *(volatile LAS u32x4*)(size_t)M_FULL;
    SCHED();
  }
}

template <int NB, int NF>
__device__ __forceinline__ void p1_mfma(const Params& P, Geo& G, rsrc_t rs, int wid, int lane, int pair0, int offF, int strB, int strF,
                                        int fstep0, int hstep0, int csf0) {
  constexpr int NT = 2 * NB + 2 * NF;
  const int g = lane >> 4, i = lane & 15, wm = wid >> 1;
  f32x4 acc[4][NT];
#pragma unroll
  for (int m = 0; m < 4; ++m)
#pragma unroll
    for (int n = 0; n < NT; ++n) acc[m][n] = (f32x4){0.f, 0.f, 0.f, 0.f};
  const int sw = (g ^ (i & 7)) * 16;
  const unsigned aA = wm * 8192 + i * 128 + sw, aB = 65536 + pair0 * 4096 + i * 128 + sw;
  const int sg = ((i >> 3) & 1) | ((g & 1) << 1);
  unsigned aF[4];
#pragma unroll
  for (int n = 0; n < 4; ++n) aF[n] = offF + (8 * g + (i >> 2)) * 128 + ((n ^ sg) * 32) + 8 * (i & 3);
  const unsigned vprog = M_PROG + wid * 4;
  bf16x8 a0[4], w0[NT], a1[4], w1[NT];
  TS(1);
  spin_ge4(M_FULL, 1u);
  TS(10); TC(58);
#pragma unroll
  for (int m = 0; m < 4; ++m) a0[m] = lds_b128(aA + m * 2048);
#pragma unroll
  for (int n = 0; n < 2 * NB; ++n) w0[n] = lds_b128(aB + n * 2048);
#pragma unroll
  for (int n = 0; n < 2 * NF; ++n) w0[2 * NB + n] = cat8(lds_tr(aF[n]), lds_tr(aF[n] + 512));
  u32x4 fl = {0u, 0u, 0u, 0u};
  int fs = 1;
#define NEXT_FS() do { fs = (fs == 6) ? 0 : fs + 1; } while (0)
#pragma unroll 1
  for (int s = 0; s < 9; ++s) {
    p1_half<NB, NF, true, false, true>(acc, a0, w0, a1, w1, (aA + (s & 3) * 16384) ^ 64, (aB + (s & 3) * strB) ^ 64, aF, fs * strF, fl, 0, vprog, 2 * s + 1);
    NEXT_FS();
    p1_half<NB, NF, true, true, false>(acc, a1, w1, a0, w0, aA + ((s + 1) & 3) * 16384, aB + ((s + 1) & 3) * strB, aF, fs * strF, fl, s + 2, vprog, 2 * s + 2);
    NEXT_FS();
  }
  p1_half<NB, NF, true, false, false>(acc, a0, w0, a1, w1, (aA + 16384) ^ 64, (aB + strB) ^ 64, aF, 5 * strF, fl, 0, vprog, 19);
  p1_half<NB, NF, false, false, false>(acc, a1, w1, a0, w0, 0, 0, aF, 0, fl, 0, vprog, 20);
#undef NEXT_FS
  TS(2); TC(59);
  spin_ge(M_SVCA, 1u);
  spin_ge(M_SVCB, 3u);
  TS(11);
  float mb[4], rd[4];
  {
    const f32x4 s0 = lds_f4(M_STAT + (64 * wm + 4 * i) * 8), s1 = lds_f4(M_STAT + (64 * wm + 4 * i) * 8 + 16);
    mb[0] = s0[0]; mb[1] = s0[2]; mb[2] = s1[0]; mb[3] = s1[2];
    rd[0] = s0[1]; rd[1] = s0[3]; rd[2] = s1[1]; rd[3] = s1[3];
  }
  const unsigned tag = lds_ld(M_TAG);
#define TABLE(k, pos) (C4){lds_f4(M_TAB + (k) * 384 + (pos) * 4), lds_f4(M_TAB + (k) * 384 + 192 + (pos) * 4)}
  C4 Ep[NB];
#pragma unroll
  for (int p = 0; p < NB; ++p) {
    const int pos = 16 * (pair0 + p) + 4 * g;
    const f32x4 csr = lds_f4(M_CS + pos * 4), csi = lds_f4(M_CS + 192 + pos * 4);
    const C4 L1 = TABLE(0, pos);
    C4 h;
#pragma unroll
    for (int m = 0; m < 4; ++m) {
      C4 bu;
      bu.re = fma4(csr, (f32x4)(-mb[m]), acc[m][2 * p]) * rd[m];
      bu.im = fma4(csi, (f32x4)(-mb[m]), acc[m][2 * p + 1]) * rd[m];
      h = (m == 0) ? bu : cfma4(L1, h, bu);
      acc[m][2 * p] = h.re; acc[m][2 * p + 1] = h.im;
    }
    C4 e = h;
    e = cfma4(TABLE(1, pos), row_shr4<1>(e), e);
    e = cfma4(TABLE(2, pos), row_shr4<2>(e), e);
    e = cfma4(TABLE(3, pos), row_shr4<4>(e), e);
    e = cfma4(TABLE(4, pos), row_shr4<8>(e), e);
    if (i == 15) {
      *(LAS f32x4*)(size_t)(M_ENDW + wm * 384 + pos * 4) = e.re;
      *(LAS f32x4*)(size_t)(M_ENDW + wm * 384 + 192 + pos * 4) = e.im;
    }
    Ep[p] = row_shr4<1>(e);
  }
  WAIT_L(0);
  lds_inc(M_CNTE);
  TS(12);
  const int afv = W_AF + (G.c * 8 + 4 * wm) * 90112 + lane * 16;
  const u32x2 flg = {tag, ~tag};
  u32x4 ap[NF > 0 ? NF : 1][4];
  if (NF > 0) {
#pragma unroll
    for (int f = 0; f < NF; ++f) {
      const f32x4 cs0 = lds_f4(M_CS + (csf0 + 32 * f + 4 * g) * 4), cs1 = lds_f4(M_CS + (csf0 + 32 * f + 16 + 4 * g) * 4);
#pragma unroll
      for (int m = 0; m < 4; ++m) {
        const u32x2 o0 = silu_pack4(fma4(cs0, (f32x4)(-mb[m]), acc[m][2 * NB + 2 * f]) * rd[m]);
        const u32x2 o1 = silu_pack4(fma4(cs1, (f32x4)(-mb[m]), acc[m][2 * NB + 2 * f + 1]) * rd[m]);
        st_sc1((u32x4){o0[0], o0[1], o1[0], o1[1]}, rs, afv + m * 90112 + f * 1024, fstep0 * 1024);
      }
    }
  }
  TS(13);
  C4 PW[NB], CL[NB][4];
#pragma unroll
  for (int p = 0; p < NB; ++p) {
    const int pos = 16 * (pair0 + p) + 4 * g;
    const C4 CF = TABLE(5, pos);
    PW[p] = (C4){lds_f4(M_PW + i * 400 + pos * 4), lds_f4(M_PW + i * 400 + 192 + pos * 4)};
#pragma unroll
    for (int m = 0; m < 4; ++m) {
      CL[p][m] = TABLE(6 + m, pos);
      const C4 l = cmul4(CF, (C4){acc[m][2 * p], acc[m][2 * p + 1]});
      acc[m][2 * p] = l.re; acc[m][2 * p + 1] = l.im;
    }
  }
  TS(14);
  WAIT_V(0);
  if (lane == 0) __builtin_amdgcn_raw_buffer_store_b64(flg, rs, W_FLAGA + ((G.c * 16 + G.pid) * 4 + wid) * 8, 0, 16);
  TS(3);
#undef TABLE
  spin_ge(M_CARRY, 1u);
  TS(4);
#pragma unroll
  for (int p = 0; p < NB; ++p) {
    const int pos = 16 * (pair0 + p) + 4 * g;
    const C4 S = {lds_f4(M_SIN + wm * 384 + pos * 4), lds_f4(M_SIN + wm * 384 + 192 + pos * 4)};
    const C4 X = cfma4(PW[p], S, Ep[p]);
#pragma unroll
    for (int m = 0; m < 4; ++m) {
      const C4 h = cfma4(CL[p][m], X, (C4){acc[m][2 * p], acc[m][2 * p + 1]});
      const u32x2 x0 = __builtin_amdgcn_permlane32_swap(pack2(h.re[0], h.re[1]), pack2(-h.im[0], -h.im[1]), false, false);
      const u32x2 x1 = __builtin_amdgcn_permlane32_swap(pack2(h.re[2], h.re[3]), pack2(-h.im[2], -h.im[3]), false, false);
      st_sc1((u32x4){x0[0], x1[0], x0[1], x1[1]}, rs, afv + m * 90112 + p * 1024, hstep0 * 1024);
    }
  }
  TS(5);
}

#define POLL_OFF 10224
template <int ROLE>
__device__ __forceinline__ void p2_mfma(const Params& P, Geo& G, rsrc_t rs, int wid, int lane, int ct, int q0) {
  constexpr int NS = ROLE ? 40 : 48, NQ = NS / 4, DP = ROLE ? 14 : 16;
  const int g = lane >> 4, i = lane & 15;
  const int tl = 64 * (wid >> 1) + 4 * i + 2 * (wid & 1);
  const GAS char* xrow = (const GAS char*)P.x + (size_t)(128 * G.c + tl) * 1280 + (80 * ct + 4 * g) * 2;
  f32x4 acc[2][5];
  const unsigned tag = lds_ld(M_TAG);
  const int vflag = W_FLAGH + ((G.c * 16 + G.pid) * 4 + wid) * 8;
#define PUB_H() do { if (lane == 0) __builtin_amdgcn_raw_buffer_store_b64((u32x2){tag, ~tag}, rs, vflag, 0, 16); } while (0)
  bool early = false;
  if (ROLE) {
    u32x2 dv[5], xv[5][2];
#pragma unroll
    for (int n = 0; n < 5; ++n) {
      dv[n] = *(const GAS u32x2*)((const GAS char*)P.dskip + (80 * ct + 16 * n + 4 * g) * 2);
#pragma unroll
      for (int m = 0; m < 2; ++m) xv[n][m] = *(const GAS u32x2*)(xrow + m * 1280 + n * 32);
    }
    WAIT_V(15);
    PUB_H();
    TS(16);
    const f32x4 st = lds_f4(M_STAT + tl * 8);
#pragma unroll
    for (int n = 0; n < 5; ++n) {
      const f32x4 df = {lo_f(dv[n][0]), hi_f(dv[n][0]), lo_f(dv[n][1]), hi_f(dv[n][1])};
#pragma unroll
      for (int m = 0; m < 2; ++m) {
        const f32x4 xf = {lo_f(xv[n][m][0]), hi_f(xv[n][m][0]), lo_f(xv[n][m][1]), hi_f(xv[n][m][1])};
#pragma unroll
        for (int e = 0; e < 4; ++e) acc[m][n][e] = 0.5f * bfr(bfr(bfr(xf[e] - st[2 * m]) * st[2 * m + 1]) * df[e]);
        asm volatile("" : "+v"(acc[m][n]));
      }
    }
  } else {
    early = __builtin_amdgcn_readfirstlane(lds_ld(M_GO)) != 0u;
    if (!early) { WAIT_V(0); PUB_H(); TS(16); }
#pragma unroll
    for (int m = 0; m < 2; ++m)
#pragma unroll
      for (int n = 0; n < 5; ++n) acc[m][n] = (f32x4){0.f, 0.f, 0.f, 0.f};
  }
  unsigned bW[4];
  if (ROLE) {
#pragma unroll
    for (int k = 0; k < 4; ++k) bW[k] = (g >> 1) * 10240 + i * 128 + (((2 * k + (g & 1)) ^ (i & 7)) * 16);
  } else bW[0] = (4 * g + (i >> 2)) * 160 + 8 * (i & 3);
  const int v0 = W_AF + (G.c * 8 + 2 * wid) * 90112 + (ROLE ? 48 * 1024 : 0) + lane * 16, v1 = v0 + 90112;
  const int gv = W_GB + ((G.c * 4 + wid) * 40 + 5 * ct) * 2048 + lane * 16;
  const unsigned vprog = M_PROG + wid * 4;
  u32x4 ra[DP][2], gq[5][2];
  u32x2 xq[5][2];
  bf16x8 wf[2][5];
  unsigned fl = 0, vb = 0;
#define P2_SOFF(s) ((((((s) >> 2) + q0) >= NQ) ? ((s) >> 2) + q0 - NQ : ((s) >> 2) + q0) * 4096)
#define P2_BASE(s) opaque(ROLE ? bW[(s) & 3] + (((s) >> 2) % 7) * SLOT2 : bW[0] + (((s) >> 2) % 7) * SLOT2)
#define P2_READ(s, n) (ROLE ? lds_b128(vb + (n) * 2048) : cat8(lds_tr(vb + ((s) & 3) * 5120 + (n) * 32), lds_tr(vb + ((s) & 3) * 5120 + 2560 + (n) * 32)))
  spin_ge(M_GO, 1u);
  TS(6);
#pragma unroll
  for (int s = 0; s < DP - 1; ++s) { ra[s][0] = ld_sc1(rs, v0 + (s & 3) * 1024, P2_SOFF(s)); ra[s][1] = ld_sc1(rs, v1 + (s & 3) * 1024, P2_SOFF(s)); }
  SCHED();
  if (!ROLE && early) { WAIT_V(2 * (DP - 1)); PUB_H(); TS(16); }
  spin_ge(M_FULL, 11u);
  vb = P2_BASE(0);
#pragma unroll
  for (int n = 0; n < 5; ++n) wf[0][n] = P2_READ(0, n);
  SCHED();
#pragma unroll
  for (int s = 0; s < NS; ++s) {
    const int r0 = ((s & 3) == 3) ? 2 : 1;
#pragma unroll
    for (int j = 0; j < 10; ++j) {
      acc[j & 1][j >> 1] = __builtin_amdgcn_mfma_f32_16x16x32_bf16(wf[s & 1][j >> 1], __builtin_bit_cast(bf16x8, ra[s % DP][j & 1]), acc[j & 1][j >> 1], 0, 0, 0);
      SCHED();
      if (j == 0 && s + 1 < NS && (ROLE || (s & 3) == 3)) vb = P2_BASE(s + 1);
      if ((s & 3) == 3 && j == 1) {
        WAIT_L(0);
        lds_st1(vprog, 21 + (s >> 2));
        TS(40 + (s >> 2));
        if (s + 1 < NS) { if (__builtin_amdgcn_readfirstlane(fl) < 12u + (s >> 2)) spin_ge(M_FULL + (((s >> 2) + 1) & 1) * 4, 12u + (s >> 2)); }
      }
      if (s + 1 < NS && j >= r0 && j < r0 + 5) wf[(s + 1) & 1][j - r0] = P2_READ(s + 1, j - r0);
      if ((s & 3) == 2 && j == 6 && s + 2 < NS) fl = lds_ld(M_FULL + (((s >> 2) + 1) & 1) * 4);
      if (j == 7 || j == 8) {
        const int sl = s - 1 + DP;
        if (sl < NS) ra[sl % DP][j - 7] = ld_sc1(rs, (j == 7 ? v0 : v1) + (sl & 3) * 1024, P2_SOFF(sl));
        else if (ROLE && s >= 28 && s < 33) xq[s - 28][j - 7] = *(const GAS u32x2*)(xrow + (j - 7) * 1280 + (s - 28) * 32);
        else if (ROLE && s >= 35) gq[s - 35][j - 7] = (u32x4){0u, ~tag, 0u, ~tag};
      }
      SCHED();
    }
  }
#undef P2_SOFF
#undef P2_BASE
#undef P2_READ
  TS(7);
  if (ROLE == 0) {
#pragma unroll
    for (int n = 0; n < 5; ++n)
#pragma unroll
      for (int m = 0; m < 2; ++m) {
        const u32x2 s = silu_pack4(acc[m][n]);
        st_sc1((u32x4){s[0], tag, s[1], tag}, rs, gv + n * 2048 + m * 1024, 0);
      }
  } else {
#pragma clang loop unroll(disable)
    for (unsigned sp = 0; sp < SPIN_CAP; ++sp) {
      bool ok = true;
#pragma unroll
      for (int n = 0; n < 5; ++n)
#pragma unroll
        for (int m = 0; m < 2; ++m) ok &= (gq[n][m][1] == tag) & (gq[n][m][3] == tag);
      if (__all(ok)) break;
      TS(18);
#pragma clang loop unroll(disable)
      for (; sp < SPIN_CAP; ++sp) {
        MEMBAR();
        const u32x4 t = ld_sc1(rs, gv - lane * 16 + POLL_OFF, 0);
        if (__builtin_amdgcn_readfirstlane(t[1]) == tag) break;
        __builtin_amdgcn_s_sleep(2);
      }
      TS(19);
      MEMBAR();
#pragma unroll
      for (int n = 0; n < 5; ++n) { gq[n][0] = ld_sc1(rs, gv + n * 2048, 0); gq[n][1] = ld_sc1(rs, gv + n * 2048 + 1024, 0); }
    }
    TS(17);
    GAS char* orow = (GAS char*)P.out + (size_t)(128 * G.c + tl) * 2560 + (80 * ct + 4 * g) * 4;
#pragma unroll
    for (int n = 0; n < 5; ++n)
#pragma unroll
      for (int m = 0; m < 2; ++m) {
        const f32x4 xf = {lo_f(xq[n][m][0]), hi_f(xq[n][m][0]), lo_f(xq[n][m][1]), hi_f(xq[n][m][1])};
        const f32x4 gf = {lo_f(gq[n][m][0]), hi_f(gq[n][m][0]), lo_f(gq[n][m][2]), hi_f(gq[n][m][2])};
        *(GAS f32x4*)(orow + m * 2560 + n * 64) = xf + (acc[m][n] * 2.f) * gf;
      }
  }
  TS(8);
}

template <int TYPE>
__device__ __forceinline__ void loader1(const Params& P, Geo& G, int H, int lane) {
  constexpr int NPB = TYPE ? 2 : 3, NCG = TYPE ? 2 : 1;
  constexpr int STRB = TYPE ? 8192 : 12288, OFFF = TYPE ? 98304 : 114688, STRF = TYPE ? 8192 : 4096;
  constexpr int NG = 4 + NPB + NCG, NS = NG + NCG;
  const int r8 = lane >> 3, c8 = lane & 7, h = H & 1, hh = H >> 1;
  const int chb = TYPE ? 384 + 32 * G.a : 48 * G.a;
  const int colb = TYPE ? 512 + 128 * G.a : 64 * G.a;
  const rsrc_t rX = mk_rsrc(P.x, 2048 * 1280), rB = mk_rsrc(hh ? P.Bim : P.Bre, 640 * 1280), rW = mk_rsrc(P.w1, 640 * 3072);
  const int vA = 4 * r8 * 1280 + ((c8 ^ r8) * 16);
  const int vB = (8 * (r8 >> 2) + (r8 & 3)) * 1280 + ((c8 ^ r8) * 16);
  const int vF = (8 * h + r8) * 3072 + ((((c8 >> 1) ^ (((r8 >> 1) & 1) | (h << 1))) << 1) | (c8 & 1)) * 16;
  const int sA = (G.c * 128 + 32 * h + hh) * 1280, sB = (chb + 4 * h) * 1280, sF = colb * 2 + 16 * hh * 3072;
  const unsigned lb = H * 1024, vfull = M_FULL + H * 4;
  auto issueF = [&](int s, int x) {
    int f = 2 * s + x; f = f >= 14 ? f - 14 : f >= 7 ? f - 7 : f;
#pragma unroll
    for (int k = 0; k < NCG; ++k) dma(rW, vF, sF + (64 * s + 32 * x) * 3072 + k * 128, lb + OFFF + f * STRF + k * 4096);
  };
  auto issueG = [&](int s) {
    const int sl = s & 3;
#pragma unroll
    for (int k = 0; k < 4; ++k) dma(rX, vA, sA + (64 * (k >> 1) + 2 * (k & 1)) * 1280 + 128 * s, lb + sl * 16384 + k * 4096);
#pragma unroll
    for (int k = 0; k < NPB; ++k) dma(rB, vB, sB + 16 * k * 1280 + 128 * s, lb + 65536 + sl * STRB + k * 4096);
    issueF(s, 0);
  };
  issueG(0);
  BAR();
  issueF(0, 1); issueG(1); issueF(1, 1); issueG(2); issueF(2, 1);
  TS(1);
  WAIT_V(2 * NS);
  lds_st1(vfull, 1u);
  TS(10);
  issueG(3);
  WAIT_V(NS + NG);
  lds_st1(vfull, 2u);
  TS(11);
  spin_prog(1);
  issueF(3, 1);
#pragma unroll 1
  for (int s = 4; s < 10; ++s) {
    spin_prog(2 * s - 6);
    TS(20 + s);
    issueG(s);
    TS(30 + s);
    WAIT_V(NS + NG);
    lds_st1(vfull, s - 1);
    TS(40 + s);
    spin_prog(2 * s - 5);
    issueF(s, 1);
  }
}
template <int ROLE>
__device__ __forceinline__ void loader2(const Params& P, Geo& G, int H, int lane, int ct, int q0) {
  constexpr int NQ = ROLE ? 10 : 12;
  const int r8 = lane >> 3, c8 = lane & 7;
  const rsrc_t r0 = ROLE ? mk_rsrc(P.Cre, 640 * 1280) : mk_rsrc(P.w2, 1536 * 1280), r1 = ROLE ? mk_rsrc(P.Cim, 640 * 1280) : r0;
  int vo[5];
  if (ROLE) vo[0] = r8 * 1280 + ((c8 ^ r8) * 16);
  else {
#pragma unroll
    for (int v = 0; v < 5; ++v) { const int t = 64 * v + lane; vo[v] = (t / 10) * 1280 + (t % 10) * 16; }
  }
  auto issue = [&](int q) {
    const int qq = q + q0 >= NQ ? q + q0 - NQ : q + q0;
    const int sl = (q >= 7 ? q - 7 : q) * SLOT2;
#pragma unroll
    for (int j = 0; j < 20; ++j) {
      if (ROLE) dma(j >= 10 ? r1 : r0, vo[0], (80 * ct + 8 * (j % 10)) * 1280 + 128 * qq, sl + j * 1024);
      else dma(r0, vo[j % 5], (128 * qq + 32 * (j / 5)) * 1280 + 160 * ct, sl + j * 1024);
    }
  };
  spin_prog(20);
  TS(12);
  int qi = H, qp = H;
#pragma unroll 1
  while (qp < NQ) {
    if (qi < NQ) {
      const int depth = (__builtin_amdgcn_readfirstlane(lds_ld(M_GO)) != 0u) ? 6 : L2_DEPTH;
      if (qi - qp < depth) {
        if (qi == qp) spin_prog(14 + qi);
        if (qi == qp || read_prog() >= (unsigned)(14 + qi)) { issue(qi); qi += 2; continue; }
      }
    }
    if (qi - qp == 2) WAIT_V(0); else if (qi - qp == 4) WAIT_V(20); else WAIT_V(40);
    lds_st1(M_FULL + H * 4, 11 + qp);
    TS(50 + qp);
    qp += 2;
  }
}

template <int NS>
__device__ __forceinline__ void service_h2(const Params& P, Geo& G, rsrc_t rs, int lane, int role) {
  const int nch = G.type ? 32 : 48, chb = G.type ? 384 + 32 * G.a : 48 * G.a;
  const int ncol = G.type ? 128 : 64, colb = G.type ? 512 + 128 * G.a : 64 * G.a;
  const int vs = W_STAT + (128 * G.c + lane) * 16;
  const int vb = W_CSB + (chb + (lane < nch ? lane : 0)) * 16;
  const int vf = W_CSF + (colb + (2 * lane < ncol ? 2 * lane : 0)) * 8;
  u32x4 s0, s1, cb, cf[10];
  s0 = ld_sc1(rs, vs, 0); s1 = ld_sc1(rs, vs + 1024, 0); cb = ld_sc1(rs, vb, 0);
#pragma unroll
  for (int k = 0; k < 10; ++k) cf[k] = ld_sc1(rs, vf + k * 12288, 0);
  SCHED();
  WAIT_V(NS + 13);
  lds_st1(M_FULL + 8, 9u);
  WAIT_V(13);
  lds_st1(M_FULL + 8, 10u);
  spin_ge(M_SVCB, 2u);
  const unsigned tag = lds_ld(M_TAG);
#pragma clang loop unroll(disable)
  for (unsigned sp = 0; sp < SPIN_CAP; ++sp) {
    bool ok = (s0[1] == tag) & (s0[3] == tag) & (s1[1] == tag) & (s1[3] == tag) & (cb[1] == tag) & (cb[3] == tag);
#pragma unroll
    for (int k = 0; k < 10; ++k) ok &= (cf[k][1] == tag) & (cf[k][3] == tag);
    if (__all(ok)) break;
    __builtin_amdgcn_s_sleep(4);
    MEMBAR();
    s0 = ld_sc1(rs, vs, 0); s1 = ld_sc1(rs, vs + 1024, 0); cb = ld_sc1(rs, vb, 0);
#pragma unroll
    for (int k = 0; k < 10; ++k) cf[k] = ld_sc1(rs, vf + k * 12288, 0);
  }
  *(LAS u32x2*)(size_t)(M_STAT + lane * 8) = (u32x2){s0[0], s0[2]};
  *(LAS u32x2*)(size_t)(M_STAT + 512 + lane * 8) = (u32x2){s1[0], s1[2]};
  if (lane < nch) {
    *(LAS unsigned*)(size_t)(M_CS + chpos(lane) * 4) = cb[0];
    *(LAS unsigned*)(size_t)(M_CS + 192 + chpos(lane) * 4) = cb[2];
  }
  if (2 * lane < ncol) {
    float c0 = 0.f, c1 = 0.f;
#pragma unroll
    for (int k = 0; k < 10; ++k) { c0 += __uint_as_float(cf[k][0]); c1 += __uint_as_float(cf[k][2]); }
    *(LAS f32x2*)(size_t)(M_CS + (96 + 2 * lane) * 4) = (f32x2){c0, c1};
  }
  WAIT_L(0);
  lds_st1(M_SVCA, 1u);
  TS(2);
  const int vfl = (role ? W_FLAGH : W_FLAGA) + G.c * 512 + lane * 8;
  spin_ge(role ? M_CARRY : M_CNTE, role ? 1u : 4u);
#pragma clang loop unroll(disable)
  for (unsigned sp = 0; sp < SPIN_CAP; ++sp) {
    const u32x2 f = __builtin_amdgcn_raw_buffer_load_b64(rs, vfl, 0, 16);
    if (__all((f[0] == tag) & (f[1] == ~tag))) break;
    __builtin_amdgcn_s_sleep(2);
    MEMBAR();
  }
  MEMBAR();
  lds_st1(M_GO, 1u);
  TS(3);
}
__device__ __forceinline__ void service_h3(const Params& P, Geo& G, rsrc_t rs, int lane) {
  const int nch = G.type ? 32 : 48, chb = G.type ? 384 + 32 * G.a : 48 * G.a;
  const int cl = lane < nch ? lane : 0, pos = chpos(cl);
  spin_ge(M_SVCB, 2u);
  const unsigned tag = lds_ld(M_TAG);
  const f32x2 L4 = {*(const LAS float*)(size_t)(M_TAB + 384 + pos * 4), *(const LAS float*)(size_t)(M_TAB + 576 + pos * 4)};
  const f32x2 L32 = {*(const LAS float*)(size_t)(M_TAB + 4 * 384 + pos * 4), *(const LAS float*)(size_t)(M_TAB + 4 * 384 + 192 + pos * 4)};
  const f32x2 L64 = cmul(L32, L32), L128 = cmul(L64, L64);
  spin_prog(14);
  if (lane < nch) {
    f32x2 pw = {1.f, 0.f};
#pragma unroll
    for (int i = 0; i < 16; ++i) {
      *(LAS float*)(size_t)(M_PW + i * 400 + pos * 4) = pw.x;
      *(LAS float*)(size_t)(M_PW + i * 400 + 192 + pos * 4) = pw.y;
      pw = cmul(pw, L4);
    }
  }
  WAIT_L(0);
  lds_inc(M_SVCB);
  TS(2);
  spin_ge(M_CNTE, 4u);
  TS(4);
  const f32x2 e0 = {*(const LAS float*)(size_t)(M_ENDW + pos * 4), *(const LAS float*)(size_t)(M_ENDW + 192 + pos * 4)};
  const f32x2 e1 = {*(const LAS float*)(size_t)(M_ENDW + 384 + pos * 4), *(const LAS float*)(size_t)(M_ENDW + 576 + pos * 4)};
  const int ve = W_EGR + (chb + cl) * 16;
  if (G.c < 15 && lane < nch) {
    const f32x2 ec = cfma(L64, e0, e1);
    st_sc1((u32x4){__float_as_uint(ec.x), tag, __float_as_uint(ec.y), tag}, rs, ve + G.c * 10240, 0);
  }
  lds_st1(M_EGS, 1u);
  f32x2 S = {0.f, 0.f};
  if (G.c > 0) {
    u32x4 ev[15];
#pragma clang loop unroll(disable)
    for (unsigned sp = 0; sp < SPIN_CAP; ++sp) {
      bool ok = true;
#pragma unroll
      for (int j = 0; j < 15; ++j) ev[j] = ld_sc1(rs, ve + (j < G.c ? j : 0) * 10240, 0);
#pragma unroll
      for (int j = 0; j < 15; ++j) ok &= (ev[j][1] == tag) & (ev[j][3] == tag);
      if (__all(ok)) break;
      __builtin_amdgcn_s_sleep(1);
      MEMBAR();
    }
#pragma unroll
    for (int j = 0; j < 15; ++j)
      if (j < G.c) S = cfma(L128, S, (f32x2){__uint_as_float(ev[j][0]), __uint_as_float(ev[j][2])});
  }
  if (lane < nch) {
    const f32x2 S1 = cfma(L64, S, e0);
    *(LAS float*)(size_t)(M_SIN + pos * 4) = S.x; *(LAS float*)(size_t)(M_SIN + 192 + pos * 4) = S.y;
    *(LAS float*)(size_t)(M_SIN + 384 + pos * 4) = S1.x; *(LAS float*)(size_t)(M_SIN + 576 + pos * 4) = S1.y;
  }
  WAIT_L(0);
  lds_st1(M_CARRY, 1u);
  TS(3);
}
template <int TYPE>
__device__ __forceinline__ void helper1(const Params& P, Geo& G, rsrc_t rs, int H, int lane, int role) {
  constexpr int NS = TYPE ? 10 : 9;
  loader1<TYPE>(P, G, H, lane);
  if (H == 2) service_h2<NS>(P, G, rs, lane, role);
  else if (H < 2) {
    const int r = G.u & 31;
    if (H == 0) {
      const rsrc_t rw = mk_rsrc(P.w2, 1536 * 1280);
      const int q = (lane * 52) >> 8;
      const int vo = lane < 60 ? q * 1280 + (lane - 5 * q) * 128 : 0x7F000000;
#pragma unroll
      for (int k = 0; k < 4; ++k) touch(rw, vo, (48 * r + 12 * k) * 1280 + 640 * G.par);
    } else {
      const rsrc_t r0 = mk_rsrc(P.Cre, 640 * 1280), r1 = mk_rsrc(P.Cim, 640 * 1280);
      const int vo = lane < 50 ? lane * 128 : 0x7F000000;
#pragma unroll
      for (int k = 0; k < 4; ++k) touch((k >> 1) ? r1 : r0, vo, (320 * G.par + 10 * r) * 1280 + (k & 1) * 6400);
    }
    WAIT_V(NS + 4);
    lds_st1(M_FULL + H * 4, 9u);
    WAIT_V(4);
    lds_st1(M_FULL + H * 4, 10u);
  }
  else {
    const int ct = 4 * G.par + (G.jj & 3);
    const int va = W_AF + (G.c * 8 + lane) * 90112 + (TYPE ? 72 + 2 * G.a : 48 + 3 * G.a) * 1024;
    const int vg = W_GB + ((G.c * 4 + lane - 8) * 40 + 5 * ct) * 2048;
    touch_sc1(rs, lane < 8 ? va : lane < 12 ? vg : 0x7F000000);
    touch(mk_rsrc(P.out, 2048 * 2560), lane < 8 ? lane * 16 * 2560 : 0x7F000000, G.c * 128 * 2560 + 320 * ct);
    WAIT_V(NS + 2);
    lds_st1(M_FULL + H * 4, 9u);
    WAIT_V(2);
    lds_st1(M_FULL + H * 4, 10u);
  }
}

extern "C" __global__ __launch_bounds__(512, 2) void s5_fused_kernel(const Params P) {
  __shared__ __attribute__((aligned(1024))) char lds_[163840];
  asm volatile("" ::"v"((LAS char*)lds_) : "memory");
  const int tid = threadIdx.x, wid = __builtin_amdgcn_readfirstlane(tid >> 6), lane = tid & 63;
  const int b = blockIdx.x, xcd = b & 7, r = b >> 3;
  Geo G;
  G.c = 4 * (xcd >> 1) + (r & 3); G.jj = r >> 2; G.type = G.jj & 1; G.par = xcd & 1;
  G.a = 4 * G.par + (G.jj >> 1); G.pid = 8 * G.par + G.jj; G.u = (xcd >> 1) * 32 + r;
  const int role = G.jj >> 2, ct = 4 * G.par + (G.jj & 3);
  const int q0 = role ? ((G.jj & 3) * 10) >> 2 : 3 * (G.jj & 3);
  const rsrc_t rs = mk_rsrc(P.ws, W_END);
  TS(0); TC(62);
  if (wid < 4) {
    if (wid == 2) {
      uint64_t pc; unsigned off, len;
      asm volatile("s_getpc_b64 %0\n.Lpf_here%=:\n\ts_mov_b32 %1, .Lpf_here%=-s5_fused_kernel\n\ts_mov_b32 %2, s5_code_end-s5_fused_kernel"
                   : "=s"(pc), "=s"(off), "=s"(len));
      len = (len < 0x40000u && off < len) ? len : 0u;
      const int per = (len >> 12) + 1;
      touch(mk_rsrc((const void*)(pc - off), len), lane < per ? (r * per + lane) * 128 : 0x7F000000, 0);
    }
    if (wid == 0) svc_rowstat(P, G, rs, lane);
    else if (wid == 1) svc_colsum(P, G, rs, lane);
    else if (wid == 2) svc_tables<0>(P, G, rs, lane);
    else svc_tables<1>(P, G, rs, lane);
    const int wn = wid & 1;
    if (G.type == 0 && wn == 0) p1_mfma<2, 0>(P, G, rs, wid, lane, 0, 114688, 12288, 4096, 0, 48 + 3 * G.a, 96);
    else {
      const int pair0 = G.type ? wn : 2;
      const int offF = G.type ? 98304 + wn * 4096 : 114688;
      const int fstep0 = G.type ? 16 + 4 * G.a + 2 * wn : 2 * G.a;
      const int hstep0 = G.type ? 72 + 2 * G.a + wn : 48 + 3 * G.a + 2;
      const int csf0 = G.type ? 96 + 64 * wn : 96;
      p1_mfma<1, 2>(P, G, rs, wid, lane, pair0, offF, G.type ? 8192 : 12288, G.type ? 8192 : 4096, fstep0, hstep0, csf0);
    }
    if (role) p2_mfma<1>(P, G, rs, wid, lane, ct, q0);
    else p2_mfma<0>(P, G, rs, wid, lane, ct, q0);
  } else {
    const int H = wid - 4;
    if (G.type) helper1<1>(P, G, rs, H, lane, role); else helper1<0>(P, G, rs, H, lane, role);
    if (H == 3) service_h3(P, G, rs, lane);
    if (H < 2) {
      if (role) loader2<1>(P, G, H, lane, ct, q0); else loader2<0>(P, G, H, lane, ct, q0);
    }
    if (b == 0 && H == 0) {
      const unsigned tag = lds_ld(M_TAG);
#pragma clang loop unroll(disable)
      for (unsigned sp = 0; sp < SPIN_CAP; ++sp) {
        bool ok = true;
#pragma unroll
        for (int k = 0; k < 4; ++k) {
          const u32x2 f = __builtin_amdgcn_raw_buffer_load_b64(rs, W_FLAGH + (lane * 4 + k) * 32, 0, 16);
          ok &= (f[0] == tag) & (f[1] == ~tag);
        }
        if (__all(ok)) break;
        __builtin_amdgcn_s_sleep(2);
        MEMBAR();
      }
      if (lane == 0) __builtin_amdgcn_raw_buffer_store_b32(tag, rs, W_EPOCH, 0, 16);
    }
  }
  TS(9); TC(63);
}
extern "C" __device__ __attribute__((used, noinline)) void s5_code_end() {}

extern "C" void kernel_launch(void* const* d_in, const int* in_sizes, int n_in, void* d_out, int out_size, void* d_ws,
                              size_t ws_size, hipStream_t stream) {
  Params P;
  P.x = (const char*)d_in[0]; P.lre = (const char*)d_in[1]; P.lim = (const char*)d_in[2];
  P.Bre = (const char*)d_in[3]; P.Bim = (const char*)d_in[4]; P.Cre = (const char*)d_in[5]; P.Cim = (const char*)d_in[6];
  P.logdt = (const char*)d_in[7]; P.dskip = (const char*)d_in[8]; P.w1 = (const char*)d_in[10]; P.w2 = (const char*)d_in[11];
  P.ws = (char*)d_ws; P.out = (float*)d_out;
  s5_fused_kernel<<<256, 512, 0, stream>>>(P);
}
```

```cpp
#include <hip/hip_runtime.h>
#include <hip/hip_bf16.h>
#include <stdint.h>

typedef __attribute__((ext_vector_type(8))) short bf16x8;
typedef __attribute__((ext_vector_type(4))) short s16x4;
typedef __attribute__((ext_vector_type(4))) float f32x4;
typedef __attribute__((ext_vector_type(2))) float f32x2;
typedef __attribute__((ext_vector_type(4))) unsigned u32x4;
typedef __attribute__((ext_vector_type(2))) unsigned u32x2;
typedef __attribute__((ext_vector_type(2))) __bf16 bf2_t;
#define GAS __attribute__((address_space(1)))
#define LAS __attribute__((address_space(3)))
typedef LAS char* lptr;

#define WAIT_V(n) asm volatile("s_waitcnt vmcnt(%0)" ::"n"(n) : "memory")
#define WAIT_L(n) asm volatile("s_waitcnt lgkmcnt(%0)" ::"n"(n) : "memory")
#define SCHED() __builtin_amdgcn_sched_barrier(0)
#define MEMBAR() asm volatile("" ::: "memory")
#define BAR() do { MEMBAR(); __builtin_amdgcn_s_barrier(); MEMBAR(); } while (0)

constexpr int W_EPOCH = 0;
constexpr int W_FLAG  = 4096;
constexpr int W_STAT  = 20480;
constexpr int W_CSB   = 53248;
constexpr int W_CSF   = 65536;
constexpr int W_EGR   = 196608;
constexpr int W_AF    = 393216;
constexpr int W_GB    = W_AF + 16 * 8 * 88 * 1024;
constexpr int W_END   = W_GB + 16 * 4 * 40 * 2048;
constexpr int W_DBG   = W_END;
constexpr int MISC   = 155648;
constexpr int M_FULL = MISC;
constexpr int M_PROG = MISC + 16;
constexpr int M_SVCA = MISC + 32, M_SVCB = MISC + 36, M_CNTE = MISC + 40, M_CARRY = MISC + 44, M_GO = MISC + 48, M_TAG = MISC + 52, M_OWN = MISC + 56;
constexpr int M_STAT = MISC + 256;
constexpr int M_CS   = MISC + 1280;
constexpr int M_ENDW = MISC + 2048;
constexpr int M_SIN  = MISC + 3328;
constexpr int M_DUMMY = MISC + 4608;
constexpr int M_TAB  = 147456;
constexpr int SLOT2  = 20480;
constexpr unsigned SPIN_CAP = 1u << 16;
#define L2_DEPTH 2

__device__ __forceinline__ float bf2f(unsigned short v) { return __uint_as_float((unsigned)v << 16); }
__device__ __forceinline__ float bfr(float a) { return (float)(__bf16)a; }
__device__ __forceinline__ unsigned pack2(float a, float b) {
  bf2_t v = {(__bf16)a, (__bf16)b};
  return __builtin_bit_cast(unsigned, v);
}
__device__ __forceinline__ float lo_f(unsigned u) { return __uint_as_float(u << 16); }
__device__ __forceinline__ float hi_f(unsigned u) { return __uint_as_float(u & 0xFFFF0000u); }
__device__ __forceinline__ f32x2 cmul(f32x2 a, f32x2 b) {
  f32x2 r; r.x = a.x * b.x - a.y * b.y; r.y = a.x * b.y + a.y * b.x; return r;
}
__device__ __forceinline__ f32x2 cfma(f32x2 a, f32x2 b, f32x2 c) {
  f32x2 r; r.x = fmaf(a.x, b.x, fmaf(-a.y, b.y, c.x)); r.y = fmaf(a.x, b.y, fmaf(a.y, b.x, c.y)); return r;
}
template <class V> struct CX { V re, im; };
typedef CX<f32x4> C4;
typedef CX<f32x2> C2;
template <class V> __device__ __forceinline__ V vfma(V a, V b, V c) { return __builtin_elementwise_fma(a, b, c); }
template <class V> __device__ __forceinline__ CX<V> cfmav(const CX<V>& a, const CX<V>& b, const CX<V>& c) {
  CX<V> r; r.re = vfma(a.re, b.re, vfma(-a.im, b.im, c.re)); r.im = vfma(a.re, b.im, vfma(a.im, b.re, c.im)); return r;
}
template <class V> __device__ __forceinline__ CX<V> cmulv(const CX<V>& a, const CX<V>& b) {
  CX<V> r; r.re = vfma(a.re, b.re, -(a.im * b.im)); r.im = vfma(a.re, b.im, a.im * b.re); return r;
}
template <int N> __device__ __forceinline__ float row_shr(float v) {
  return __builtin_bit_cast(float, __builtin_amdgcn_update_dpp(0, __builtin_bit_cast(int, v), 0x110 + N, 0xf, 0xf, true));
}
template <int N, class V> __device__ __forceinline__ CX<V> row_shrv(const CX<V>& v) {
  CX<V> r;
#pragma unroll
  for (int e = 0; e < (int)(sizeof(V) / 4); ++e) { r.re[e] = row_shr<N>(v.re[e]); r.im[e] = row_shr<N>(v.im[e]); }
  return r;
}
template <class V> __device__ __forceinline__ CX<V> lds_cx(unsigned a) {
  CX<V> r; r.re = *(const LAS V*)(size_t)a; r.im = *(const LAS V*)(size_t)(a + 320); return r;
}
template <class V> __device__ __forceinline__ CX<V> csel1(bool c, const CX<V>& a) {
  CX<V> r; r.re = c ? a.re : (V)(1.f); r.im = c ? a.im : (V)(0.f); return r;
}
__device__ __forceinline__ unsigned opaque(unsigned v) { asm volatile("" : "+v"(v)); return v; }
__device__ __forceinline__ float silu_k() { float k = -1.44269504088896341f; asm volatile("" : "+s"(k)); return k; }
__device__ __forceinline__ u32x2 silu_pack4(f32x4 v, float k) {
  const unsigned p0 = opaque(pack2(v[0], v[1])), p1 = opaque(pack2(v[2], v[3]));
  const f32x4 x = {lo_f(p0), hi_f(p0), lo_f(p1), hi_f(p1)};
  const f32x4 t = x * k;
  f32x4 d;
#pragma unroll
  for (int e = 0; e < 4; ++e) d[e] = __builtin_amdgcn_exp2f(t[e]);
  d = d + 1.f;
#pragma unroll
  for (int e = 0; e < 4; ++e) d[e] = __builtin_amdgcn_rcpf(d[e]);
  const f32x4 s = x * d;
  return (u32x2){pack2(s[0], s[1]), pack2(s[2], s[3])};
}
__device__ __forceinline__ float sum8(u32x4 v) {
  return ((lo_f(v[0]) + hi_f(v[0])) + (lo_f(v[1]) + hi_f(v[1]))) + ((lo_f(v[2]) + hi_f(v[2])) + (lo_f(v[3]) + hi_f(v[3])));
}
typedef __amdgpu_buffer_rsrc_t rsrc_t;
__device__ __forceinline__ rsrc_t mk_rsrc(const void* p, int bytes) { return __builtin_amdgcn_make_buffer_rsrc((void*)p, 0, bytes, 0x00020000); }
__device__ __forceinline__ void dma(rsrc_t r, int voff, int soff, unsigned l) {
  __builtin_amdgcn_raw_ptr_buffer_load_lds(r, (LAS void*)(size_t)l, 16, voff, soff, 0, 0);
}
__device__ __forceinline__ void touch(rsrc_t r, int voff, int soff) {
  __builtin_amdgcn_raw_ptr_buffer_load_lds(r, (LAS void*)(size_t)M_DUMMY, 4, voff, soff, 0, 0);
}
__device__ __forceinline__ void touch_sc1(rsrc_t r, int voff) {
  __builtin_amdgcn_raw_ptr_buffer_load_lds(r, (LAS void*)(size_t)M_DUMMY, 4, voff, 0, 0, 16);
}
__device__ __forceinline__ bf16x8 lds_b128(unsigned a) { return *(const LAS bf16x8*)(size_t)a; }
__device__ __forceinline__ f32x4 lds_f4(unsigned a) { return *(const LAS f32x4*)(size_t)a; }
__device__ __forceinline__ s16x4 lds_tr(unsigned a) { return __builtin_amdgcn_ds_read_tr16_b64_v4i16((LAS s16x4*)(size_t)a); }
__device__ __forceinline__ bf16x8 cat8(s16x4 a, s16x4 b) { return __builtin_shufflevector(a, b, 0, 1, 2, 3, 4, 5, 6, 7); }
__device__ __forceinline__ u32x4 ld_sc1(rsrc_t r, int voff, int soff) { return __builtin_amdgcn_raw_buffer_load_b128(r, voff, soff, 16); }
__device__ __forceinline__ void st_sc1(u32x4 v, rsrc_t r, int voff, int soff) { __builtin_amdgcn_raw_buffer_store_b128(v, r, voff + soff, 0, 16); }
__device__ __forceinline__ unsigned lds_ld(unsigned a) { return *(volatile LAS unsigned*)(size_t)a; }
__device__ __forceinline__ void lds_st1(unsigned a, unsigned v) {
  asm volatile("s_mov_b64 exec, 1\n\tds_write_b32 %0, %1\n\ts_mov_b64 exec, -1" ::"v"(a), "v"(v) : "memory");
}
__device__ __forceinline__ void spin_ge(unsigned a, unsigned need) {
#pragma clang loop unroll(disable)
  for (unsigned s = 0; s < (1u << 18); ++s) {
    if (__builtin_amdgcn_readfirstlane(lds_ld(a)) >= need) break;
    __builtin_amdgcn_s_sleep(1);
  }
  MEMBAR();
}
__device__ __forceinline__ unsigned spin_val(unsigned a, unsigned need) {
  unsigned v = 0u;
#pragma clang loop unroll(disable)
  for (unsigned s = 0; s < (1u << 18); ++s) {
    v = __builtin_amdgcn_readfirstlane(lds_ld(a));
    if (v >= need) break;
    __builtin_amdgcn_s_sleep(1);
  }
  MEMBAR();
  return v;
}
__device__ __forceinline__ unsigned min4(u32x4 p) {
  const unsigned a = p[0] < p[1] ? p[0] : p[1], b = p[2] < p[3] ? p[2] : p[3];
  return a < b ? a : b;
}
__device__ __forceinline__ void spin_ge4(unsigned a, unsigned need) {
#pragma clang loop unroll(disable)
  for (unsigned s = 0; s < (1u << 18); ++s) {
    if (__builtin_amdgcn_readfirstlane(min4(*(volatile LAS u32x4*)(size_t)a)) >= need) break;
    __builtin_amdgcn_s_sleep(1);
  }
  MEMBAR();
}
__device__ __forceinline__ float dsum2(unsigned v, float c) {
  const bf2_t one = {(__bf16)1.f, (__bf16)1.f};
  return __builtin_amdgcn_fdot2_f32_bf16(__builtin_bit_cast(bf2_t, v), one, c, false);
}
__device__ __forceinline__ unsigned read_prog() {
  const u32x4 p = *(volatile LAS u32x4*)(size_t)M_PROG;
  const unsigned a = p[0] < p[1] ? p[0] : p[1], b = p[2] < p[3] ? p[2] : p[3];
  return __builtin_amdgcn_readfirstlane(a < b ? a : b);
}
__device__ __forceinline__ void spin_prog(unsigned need) {
#pragma clang loop unroll(disable)
  for (unsigned s = 0; s < (1u << 18); ++s) {
    const u32x4 p = *(volatile LAS u32x4*)(size_t)M_PROG;
    const unsigned a = p[0] < p[1] ? p[0] : p[1], b = p[2] < p[3] ? p[2] : p[3];
    if (__builtin_amdgcn_readfirstlane(a < b ? a : b) >= need) break;
    __builtin_amdgcn_s_sleep(1);
  }
  MEMBAR();
}
__device__ __forceinline__ void lds_inc(unsigned a) {
  if ((threadIdx.x & 63) == 0) __hip_atomic_fetch_add((LAS unsigned*)(size_t)a, 1u, __ATOMIC_RELAXED, __HIP_MEMORY_SCOPE_WORKGROUP);
}
#define TS(k) do {} while (0)
#define TC(k) do {} while (0)

struct Params {
  const char *x, *lre, *lim, *Bre, *Bim, *Cre, *Cim, *logdt, *dskip, *w1, *w2;
  char* ws;
  float* out;
};
struct Geo {
  int c, role, a, pid, par, u;
};

__device__ __forceinline__ unsigned ld_epoch(rsrc_t rs) { return __builtin_amdgcn_raw_buffer_load_b32(rs, W_EPOCH, 0, 16); }
__device__ __forceinline__ int chpos(int lc) {
  const int w = lc & 15;
  return (lc >> 4) == 2 ? lc : (lc & ~15) + 4 * (2 * ((w >> 2) & 1) + (w >> 3)) + (w & 3);
}
__device__ __forceinline__ void svc_rows(const Params& P, Geo& G, rsrc_t rs, int lane, int part) {
  const int r8 = lane >> 3, c8 = lane & 7;
  const unsigned ep = ld_epoch(rs);
  const int row = 128 * G.c + 16 * G.a + 8 * part + r8;
  const int jb = 2 * ((G.u >> 5) * 16 + (G.u & 15)) + part + 128 * (r8 >> 1);
  const bool bok = (r8 < 6) && (jb < 320);
  const int chs = (bok ? jb : 0) + 320 * G.par;
  u32x4 v[10], w[10];
  const GAS char* src = (const GAS char*)P.x + (size_t)row * 1280 + c8 * 16;
  const GAS char* srb = (const GAS char*)((r8 & 1) ? P.Bim : P.Bre) + (size_t)chs * 1280 + c8 * 16;
#pragma unroll
  for (int q = 0; q < 10; ++q) v[q] = *(const GAS u32x4*)(src + q * 128);
#pragma unroll
  for (int q = 0; q < 10; ++q) w[q] = *(const GAS u32x4*)(srb + q * 128);
  if (part == 0) { if (lane < 16) *(LAS unsigned*)(size_t)(MISC + lane * 4) = 0u; WAIT_L(0); }
  BAR();
  const unsigned tag = ep + 1u;
  {
    float s0 = 0.f, s1 = 0.f;
#pragma unroll
    for (int q = 0; q < 10; ++q) { s0 = dsum2(v[q][0], s0); s1 = dsum2(v[q][1], s1); s0 = dsum2(v[q][2], s0); s1 = dsum2(v[q][3], s1); }
    float s = s0 + s1;
    s += __shfl_xor(s, 1); s += __shfl_xor(s, 2); s += __shfl_xor(s, 4);
    const float mean = s * (1.f / 640.f);
    f32x2 q0 = {0.f, 0.f}, q1 = {0.f, 0.f};
#pragma unroll
    for (int q = 0; q < 10; ++q)
#pragma unroll
      for (int e = 0; e < 4; e += 2) {
        const f32x2 a = (f32x2){lo_f(v[q][e]), hi_f(v[q][e])} - mean, b = (f32x2){lo_f(v[q][e + 1]), hi_f(v[q][e + 1])} - mean;
        q0 = __builtin_elementwise_fma(a, a, q0); q1 = __builtin_elementwise_fma(b, b, q1);
      }
    float ss = (q0[0] + q0[1]) + (q1[0] + q1[1]);
    ss += __shfl_xor(ss, 1); ss += __shfl_xor(ss, 2); ss += __shfl_xor(ss, 4);
    const float mb = bfr(mean);
    const float den = bfr(sqrtf(bfr(bfr(ss * (1.f / 640.f)) + 1e-5f)));
    if (c8 == 0) st_sc1((u32x4){__float_as_uint(mb), tag, __float_as_uint(1.f / den), tag}, rs, W_STAT + row * 16, 0);
  }
  float s0 = 0.f, s1 = 0.f;
#pragma unroll
  for (int q = 0; q < 10; ++q) { s0 = dsum2(w[q][0], s0); s1 = dsum2(w[q][1], s1); s0 = dsum2(w[q][2], s0); s1 = dsum2(w[q][3], s1); }
  float s = s0 + s1;
  s += __shfl_xor(s, 1); s += __shfl_xor(s, 2); s += __shfl_xor(s, 4);
  const float si = __shfl_xor(s, 8);
  if (bok && (lane & 15) == 0) st_sc1((u32x4){__float_as_uint(s), tag, __float_as_uint(si), tag}, rs, W_CSB + chs * 16, 0);
}
__device__ __forceinline__ void svc_tables(const Params& P, Geo& G, rsrc_t rs, int lane, int part) {
  const int cr = lane + 64 * part, cl = cr < 80 ? cr : 0, pos = chpos(cl), chp = 80 * G.a + cl;
  const unsigned short wl = *(const GAS unsigned short*)((const GAS char*)P.logdt + chp * 2);
  const unsigned short wr = *(const GAS unsigned short*)((const GAS char*)P.lre + chp * 2);
  const unsigned short wi = *(const GAS unsigned short*)((const GAS char*)P.lim + chp * 2);
  const unsigned ep = ld_epoch(rs);
  const int r8 = lane >> 3, c8 = lane & 7;
  const int unit = 2 * ((G.u >> 5) * 16 + (G.u & 15)) + part;
  const int uu = unit < 120 ? unit : 0;
  const int cg = uu % 12 + 12 * G.par, kr = uu / 12;
  u32x4 f[8];
  const GAS char* src = (const GAS char*)P.w1 + (size_t)(64 * kr + r8) * 3072 + cg * 128 + c8 * 16;
#pragma unroll
  for (int q = 0; q < 8; ++q) f[q] = *(const GAS u32x4*)(src + q * 8 * 3072);
  BAR();
  const float dt = bfr(expf(bf2f(wl)));
  const float ar = bf2f(wr), ai = bf2f(wi);
  const float zr = dt * ar, zi = dt * ai;
  const float ex = expf(zr);
  float sn, cs;
  sincosf(zi, &sn, &cs);
  const f32x2 L = {ex * cs, ex * sn};
  const f32x2 L2 = cmul(L, L), L4 = cmul(L2, L2);
  f32x2 tb[10];
  tb[0] = L; tb[1] = L4; tb[2] = cmul(L4, L4); tb[3] = cmul(tb[2], tb[2]); tb[4] = cmul(tb[3], tb[3]);
  const float nr = L.x - 1.f, ni = L.y, dd = ar * ar + ai * ai;
  const f32x2 CF = {(nr * ar + ni * ai) / dd, (ni * ar - nr * ai) / dd};
  tb[5] = CF; tb[6] = cmul(CF, L); tb[7] = cmul(CF, L2); tb[8] = cmul(CF, cmul(L2, L)); tb[9] = cmul(CF, L4);
  if (cr < 80) {
#pragma unroll
    for (int k = 0; k < 10; ++k) {
      *(LAS float*)(size_t)(M_TAB + k * 640 + pos * 4) = tb[k].x;
      *(LAS float*)(size_t)(M_TAB + k * 640 + 320 + pos * 4) = tb[k].y;
    }
  }
  const unsigned tag = ep + 1u;
  if (part) lds_st1(M_TAG, tag);
  WAIT_L(0);
  lds_inc(M_SVCB);
  SCHED();
  float cq[8];
#pragma unroll
  for (int e = 0; e < 4; ++e) {
    f32x2 a = {0.f, 0.f};
#pragma unroll
    for (int q = 0; q < 8; ++q) a += (f32x2){lo_f(f[q][e]), hi_f(f[q][e])};
    cq[2 * e] = a[0]; cq[2 * e + 1] = a[1];
  }
#pragma unroll
  for (int e = 0; e < 8; ++e) { cq[e] += __shfl_xor(cq[e], 8); cq[e] += __shfl_xor(cq[e], 16); cq[e] += __shfl_xor(cq[e], 32); }
  const int rs4 = r8 & 3;
  const float o0 = rs4 == 0 ? cq[0] : rs4 == 1 ? cq[2] : rs4 == 2 ? cq[4] : cq[6];
  const float o1 = rs4 == 0 ? cq[1] : rs4 == 1 ? cq[3] : rs4 == 2 ? cq[5] : cq[7];
  if (unit < 120 && r8 < 4) st_sc1((u32x4){__float_as_uint(o0), tag, __float_as_uint(o1), tag}, rs, W_CSF + kr * 12288 + (64 * cg + 8 * c8 + 2 * rs4) * 8, 0);
}

struct Tabs { C4 t[2][5]; C2 s[5]; };
template <int K0> __device__ __forceinline__ void tab_rd(Tabs& T, int k, unsigned p4, unsigned p2) {
  if (k < 10) T.t[k / 5][k % 5] = lds_cx<f32x4>(M_TAB + (K0 + k % 5) * 640 + 64 * (k / 5) + p4);
  else T.s[k - 10] = lds_cx<f32x2>(M_TAB + (K0 + k - 10) * 640 + p2);
}
template <int ROLE, bool RD, bool CHK, bool PRE>
__device__ __forceinline__ void p1_half(f32x4 (&acc)[4][6 - ROLE], const bf16x8 (&ca)[4], const bf16x8 (&cw)[6 - ROLE], bf16x8 (&na)[4],
                                        bf16x8 (&nw)[6 - ROLE], unsigned vA, unsigned vB, unsigned dM, const unsigned (&aF)[6], unsigned fo,
                                        u32x4& fl, unsigned need, unsigned vprog, unsigned prog, Tabs& T) {
  constexpr int NT = 6 - ROLE;
  s16x4 t0;
#pragma unroll
  for (int j = 0; j < 4 * NT; ++j) {
    acc[j & 3][j >> 2] = __builtin_amdgcn_mfma_f32_16x16x32_bf16(cw[j >> 2], ca[j & 3], acc[j & 3][j >> 2], 0, 0, 0);
    SCHED();
    if (j == 1) {
      WAIT_L(0);
      unsigned z = 0;
      if (CHK) z = __builtin_amdgcn_readfirstlane(min4(fl));
      SCHED();
      lds_st1(vprog, prog);
      if (CHK) { if (z < need) spin_ge4(M_FULL, need); }
      SCHED();
    }
    if (RD) {
      if (j >= 2 && j < 6) na[j - 2] = lds_b128(vA + (j - 2) * 2048);
      else if (ROLE) {
        if (j >= 6 && j < 10) nw[j - 6] = lds_b128(vB + (j - 6) * 2048);
        else if (j == 10) nw[4] = lds_b128(vB + dM);
      } else if (j >= 6 && j < 18) {
        const unsigned q = aF[(j - 6) >> 1] + fo;
        if (((j - 6) & 1) == 0) t0 = lds_tr(q);
        else nw[(j - 6) >> 1] = cat8(t0, lds_tr(q + 512));
      }
    }
    if (ROLE && !RD && j >= 2 && j < 17) tab_rd<0>(T, j - 2, vA, vB);
    if (PRE && j == 4 * NT - 5) fl = *(volatile LAS u32x4*)(size_t)M_FULL;
    SCHED();
  }
}
template <class V>
__device__ __forceinline__ void scan_local(CX<V> (&h)[4], CX<V>& e, const CX<V> (&t)[5]) {
#pragma unroll
  for (int m = 1; m < 4; ++m) h[m] = cfmav(t[0], h[m - 1], h[m]);
  e = h[3];
  e = cfmav(t[1], row_shrv<1>(e), e);
  e = cfmav(t[2], row_shrv<2>(e), e);
  e = cfmav(t[3], row_shrv<4>(e), e);
  e = cfmav(t[4], row_shrv<8>(e), e);
}
template <class V> __device__ __forceinline__ CX<V> lane_pow(const CX<V> (&t)[5], int i) {
  return cmulv(cmulv(csel1(i & 1, t[1]), csel1(i & 2, t[2])), cmulv(csel1(i & 4, t[3]), csel1(i & 8, t[4])));
}
template <class V>
__device__ __forceinline__ void pre_carry(CX<V> (&h)[4], CX<V> (&Q)[4], const CX<V>& Ep, const CX<V>& PW, const CX<V> (&c)[5]) {
#pragma unroll
  for (int m = 0; m < 4; ++m) {
    h[m] = cfmav(c[1 + m], Ep, cmulv(c[0], h[m]));
    Q[m] = cmulv(c[1 + m], PW);
    asm volatile("" : "+v"(h[m].re), "+v"(h[m].im), "+v"(Q[m].re), "+v"(Q[m].im));
  }
}

template <int ROLE>
__device__ __forceinline__ void p1_mfma(const Params& P, Geo& G, rsrc_t rs, int wid, int lane) {
  constexpr int NT = 6 - ROLE, STRB = 20480, STRF = 12288;
  const int g = lane >> 4, i = lane & 15, wm = wid >> 1, wn = wid & 1;
  f32x4 acc[4][NT];
#pragma unroll
  for (int m = 0; m < 4; ++m)
#pragma unroll
    for (int n = 0; n < NT; ++n) acc[m][n] = (f32x4){0.f, 0.f, 0.f, 0.f};
  const int sw = (g ^ (i & 7)) * 16;
  const unsigned aA = wm * 8192 + i * 128 + sw, aB = 65536 + wn * 12288 + i * 128 + sw;
  const unsigned dM = (65536 + 8192 + wn * 1024 + (i & 7) * 128 + (i >> 3) * 2048 + sw) - aB;
  const int sg = ((i >> 3) & 1) | ((g & 1) << 1);
  unsigned aF[6];
#pragma unroll
  for (int n = 0; n < 6; ++n) {
    const int t = 6 * wn + n;
    aF[n] = 65536 + (t >> 2) * 4096 + (8 * g + (i >> 2)) * 128 + (((t & 3) ^ sg) * 32) + 8 * (i & 3);
  }
  const unsigned vprog = M_PROG + wid * 4;
  bf16x8 a0[4], w0[NT], a1[4], w1[NT];
  Tabs T;
  const unsigned p4 = (48 * wn + 4 * g) * 4, p2 = (32 + 8 * wn + 4 * (g & 1) + 2 * (g >> 1)) * 4;
  TS(1);
  spin_ge4(M_FULL, 1u);
  TS(10); TC(58);
#pragma unroll
  for (int m = 0; m < 4; ++m) a0[m] = lds_b128(aA + m * 2048);
  if (ROLE) {
#pragma unroll
    for (int n = 0; n < 4; ++n) w0[n] = lds_b128(aB + n * 2048);
    w0[4] = lds_b128(aB + dM);
  } else {
#pragma unroll
    for (int n = 0; n < NT; ++n) w0[n] = cat8(lds_tr(aF[n]), lds_tr(aF[n] + 512));
  }
  u32x4 fl = {0u, 0u, 0u, 0u};
  int fs = 1;
#define NEXT_FS() do { fs = (fs == 6) ? 0 : fs + 1; } while (0)
#pragma unroll 1
  for (int s = 0; s < 9; ++s) {
    p1_half<ROLE, true, false, true>(acc, a0, w0, a1, w1, (aA + (s & 3) * 16384) ^ 64, (aB + (s & 3) * STRB) ^ 64, dM, aF, fs * STRF, fl, 0, vprog, 2 * s + 1, T);
    NEXT_FS();
    p1_half<ROLE, true, true, false>(acc, a1, w1, a0, w0, aA + ((s + 1) & 3) * 16384, aB + ((s + 1) & 3) * STRB, dM, aF, fs * STRF, fl, s + 2, vprog, 2 * s + 2, T);
    NEXT_FS();
  }
  if (ROLE) spin_ge(M_SVCB, 2u);
  p1_half<ROLE, true, false, false>(acc, a0, w0, a1, w1, (aA + 16384) ^ 64, (aB + STRB) ^ 64, dM, aF, 5 * STRF, fl, 0, vprog, 19, T);
  p1_half<ROLE, false, false, false>(acc, a1, w1, a0, w0, p4, p2, dM, aF, 0, fl, 0, vprog, 20, T);
#undef NEXT_FS
  TS(2); TC(59);
  spin_ge(M_SVCA, ROLE ? 1u : 2u);
  TS(11);
  float mb[4], rd[4];
  {
    const f32x4 s0 = lds_f4(M_STAT + (64 * wm + 4 * i) * 8), s1 = lds_f4(M_STAT + (64 * wm + 4 * i) * 8 + 16);
    mb[0] = s0[0]; mb[1] = s0[2]; mb[2] = s1[0]; mb[3] = s1[2];
    rd[0] = s0[1]; rd[1] = s0[3]; rd[2] = s1[1]; rd[3] = s1[3];
  }
  const int afv = W_AF + (G.c * 8 + 4 * wm) * 90112 + lane * 16;
  if (ROLE == 0) {
    const int av = afv + (6 * G.a + 3 * wn) * 1024;
    const float kk = silu_k();
#pragma unroll
    for (int f = 0; f < 3; ++f) {
      const f32x4 cs0 = lds_f4(M_CS + (96 * wn + 32 * f + 4 * g) * 4), cs1 = lds_f4(M_CS + (96 * wn + 32 * f + 16 + 4 * g) * 4);
#pragma unroll
      for (int m = 0; m < 4; ++m) {
        const u32x2 o0 = silu_pack4(vfma(cs0, (f32x4)(-mb[m]), acc[m][2 * f]) * rd[m], kk);
        const u32x2 o1 = silu_pack4(vfma(cs1, (f32x4)(-mb[m]), acc[m][2 * f + 1]) * rd[m], kk);
        st_sc1((u32x4){o0[0], o0[1], o1[0], o1[1]}, rs, av + m * 90112 + f * 1024, 0);
      }
    }
    lds_inc(M_CNTE);
    TS(12);
  } else {
    C4 h[2][4], e[2], Q[2][4];
    C2 hs[4], es, Qs[4];
    {
      const C4 c0 = lds_cx<f32x4>(M_CS + p4), c1 = lds_cx<f32x4>(M_CS + p4 + 64);
      const f32x4 cs = lds_f4(M_CS + (g >> 1) * 320 + (32 + 8 * wn + 4 * (g & 1)) * 4);
#pragma unroll
      for (int m = 0; m < 4; ++m) {
        h[0][m].re = vfma(c0.re, (f32x4)(-mb[m]), acc[m][0]) * rd[m];
        h[0][m].im = vfma(c0.im, (f32x4)(-mb[m]), acc[m][1]) * rd[m];
        h[1][m].re = vfma(c1.re, (f32x4)(-mb[m]), acc[m][2]) * rd[m];
        h[1][m].im = vfma(c1.im, (f32x4)(-mb[m]), acc[m][3]) * rd[m];
        const u32x4 b = __builtin_bit_cast(u32x4, vfma(cs, (f32x4)(-mb[m]), acc[m][4]) * rd[m]);
        const u32x2 x0 = __builtin_amdgcn_permlane32_swap(b[0], b[2], false, false);
        const u32x2 x1 = __builtin_amdgcn_permlane32_swap(b[1], b[3], false, false);
        hs[m].re = (f32x2){__uint_as_float(x0[0]), __uint_as_float(x1[0])};
        hs[m].im = (f32x2){__uint_as_float(x0[1]), __uint_as_float(x1[1])};
      }
    }
    scan_local(h[0], e[0], T.t[0]);
    scan_local(h[1], e[1], T.t[1]);
    scan_local(hs, es, T.s);
    if (i == 15) {
      const unsigned w4 = M_ENDW + wm * 640 + p4, w2 = M_ENDW + wm * 640 + p2;
      *(LAS f32x4*)(size_t)w4 = e[0].re; *(LAS f32x4*)(size_t)(w4 + 320) = e[0].im;
      *(LAS f32x4*)(size_t)(w4 + 64) = e[1].re; *(LAS f32x4*)(size_t)(w4 + 384) = e[1].im;
      *(LAS f32x2*)(size_t)w2 = es.re; *(LAS f32x2*)(size_t)(w2 + 320) = es.im;
    }
    WAIT_L(0);
    lds_inc(M_CNTE);
    TS(12);
    SCHED();
    {
      C4 pw0 = lane_pow(T.t[0], i), pw1 = lane_pow(T.t[1], i);
      C2 pws = lane_pow(T.s, i);
      asm volatile("" : "+v"(pw0.re), "+v"(pw0.im), "+v"(pw1.re), "+v"(pw1.im), "+v"(pws.re), "+v"(pws.im));
      SCHED();
      Tabs U;
#pragma unroll
      for (int k = 0; k < 10; ++k) tab_rd<5>(U, k, p4, p2);
      SCHED();
      pre_carry(h[0], Q[0], row_shrv<1>(e[0]), pw0, U.t[0]);
      SCHED();
#pragma unroll
      for (int k = 10; k < 15; ++k) tab_rd<5>(U, k, p4, p2);
      SCHED();
      pre_carry(h[1], Q[1], row_shrv<1>(e[1]), pw1, U.t[1]);
      pre_carry(hs, Qs, row_shrv<1>(es), pws, U.s);
    }
    const int hv = afv + (48 + 5 * G.a + 3 * wn) * 1024;
    const int hsv = afv - lane * 16 + (48 + 5 * G.a + 2) * 1024 + ((wn + 2 * (g >> 1)) * 16 + i) * 16 + (g & 1) * 8;
    TS(3);
    spin_ge(M_CARRY, 2u);
    TS(4);
#pragma unroll
    for (int p = 0; p < 2; ++p) {
      const C4 S = lds_cx<f32x4>(M_SIN + wm * 640 + p4 + 64 * p);
#pragma unroll
      for (int m = 0; m < 4; ++m) {
        const C4 y = cfmav(Q[p][m], S, h[p][m]);
        const u32x2 x0 = __builtin_amdgcn_permlane32_swap(pack2(y.re[0], y.re[1]), pack2(-y.im[0], -y.im[1]), false, false);
        const u32x2 x1 = __builtin_amdgcn_permlane32_swap(pack2(y.re[2], y.re[3]), pack2(-y.im[2], -y.im[3]), false, false);
        st_sc1((u32x4){x0[0], x1[0], x0[1], x1[1]}, rs, hv + m * 90112 + p * 1024, 0);
      }
    }
    {
      const C2 S = lds_cx<f32x2>(M_SIN + wm * 640 + p2);
#pragma unroll
      for (int m = 0; m < 4; ++m) {
        const C2 y = cfmav(Qs[m], S, hs[m]);
        const u32x2 x0 = __builtin_amdgcn_permlane32_swap(pack2(y.re[0], y.re[1]), pack2(-y.im[0], -y.im[1]), false, false);
        __builtin_amdgcn_raw_buffer_store_b64(x0, rs, hsv + m * 90112, 0, 16);
      }
    }
    TS(5);
  }
}

#define POLL_OFF 10224
template <int ROLE>
__device__ __forceinline__ void p2_mfma(const Params& P, Geo& G, rsrc_t rs, int wid, int lane, int ct, int q0) {
  constexpr int NS = ROLE ? 40 : 48, NQ = NS / 4, DP = ROLE ? 14 : 16;
  const int g = lane >> 4, i = lane & 15;
  const int tl = 64 * (wid >> 1) + 4 * i + 2 * (wid & 1);
  const GAS char* xrow = (const GAS char*)P.x + (size_t)(128 * G.c + tl) * 1280 + (80 * ct + 4 * g) * 2;
  f32x4 acc[2][5];
  const unsigned tag = lds_ld(M_TAG);
  const int vflag = W_FLAG + (((G.c * 2 + ROLE) * 8 + G.a) * 4 + wid) * 8;
#define PUB_H() do { if (lane == 0) __builtin_amdgcn_raw_buffer_store_b64((u32x2){tag, ~tag}, rs, vflag, 0, 16); } while (0)
  if (ROLE) {
    u32x2 dv[5], xv[5][2];
#pragma unroll
    for (int n = 0; n < 5; ++n) {
      dv[n] = *(const GAS u32x2*)((const GAS char*)P.dskip + (80 * ct + 16 * n + 4 * g) * 2);
#pragma unroll
      for (int m = 0; m < 2; ++m) xv[n][m] = *(const GAS u32x2*)(xrow + m * 1280 + n * 32);
    }
    WAIT_V(15);
    PUB_H();
    lds_inc(M_OWN);
    TS(16);
    const f32x4 st = lds_f4(M_STAT + tl * 8);
#pragma unroll
    for (int n = 0; n < 5; ++n) {
      const f32x4 df = {lo_f(dv[n][0]), hi_f(dv[n][0]), lo_f(dv[n][1]), hi_f(dv[n][1])};
#pragma unroll
      for (int m = 0; m < 2; ++m) {
        const f32x4 xf = {lo_f(xv[n][m][0]), hi_f(xv[n][m][0]), lo_f(xv[n][m][1]), hi_f(xv[n][m][1])};
#pragma unroll
        for (int e = 0; e < 4; ++e) acc[m][n][e] = 0.5f * bfr(bfr(bfr(xf[e] - st[2 * m]) * st[2 * m + 1]) * df[e]);
        asm volatile("" : "+v"(acc[m][n]));
      }
    }
  } else {
    WAIT_V(0);
    PUB_H();
    lds_inc(M_OWN);
    TS(16);
#pragma unroll
    for (int m = 0; m < 2; ++m)
#pragma unroll
      for (int n = 0; n < 5; ++n) acc[m][n] = (f32x4){0.f, 0.f, 0.f, 0.f};
  }
  unsigned bW[4];
  if (ROLE) {
#pragma unroll
    for (int k = 0; k < 4; ++k) bW[k] = (g >> 1) * 10240 + i * 128 + (((2 * k + (g & 1)) ^ (i & 7)) * 16);
  } else bW[0] = (4 * g + (i >> 2)) * 160 + 8 * (i & 3);
  const int v0 = W_AF + (G.c * 8 + 2 * wid) * 90112 + (ROLE ? 48 * 1024 : 0) + lane * 16, v1 = v0 + 90112;
  const int gv = W_GB + ((G.c * 4 + wid) * 40 + 5 * ct) * 2048 + lane * 16;
  const unsigned vprog = M_PROG + wid * 4;
  u32x4 ra[DP][2], gq[5][2];
  u32x2 xq[5][2];
  bf16x8 wf[2][5];
  unsigned fl = 0, vb = 0;
#define P2_SOFF(s) ((((((s) >> 2) + q0) >= NQ) ? ((s) >> 2) + q0 - NQ : ((s) >> 2) + q0) * 4096)
#define P2_BASE(s) opaque(ROLE ? bW[(s) & 3] + (((s) >> 2) % 7) * SLOT2 : bW[0] + (((s) >> 2) % 7) * SLOT2)
#define P2_READ(s, n) (ROLE ? lds_b128(vb + (n) * 2048) : cat8(lds_tr(vb + ((s) & 3) * 5120 + (n) * 32), lds_tr(vb + ((s) & 3) * 5120 + 2560 + (n) * 32)))
  unsigned rdy = (ROLE ? 5 : 6) * (G.a + 1) - 4 * q0;
#define P2_RDY(s) do { if ((unsigned)(s) >= rdy) rdy = spin_val(M_GO, (s) + 1u); } while (0)
  spin_ge(M_OWN, 4u);
  TS(6);
#pragma unroll
  for (int s = 0; s < DP - 1; ++s) {
    if (s >= 2) P2_RDY(s);
    ra[s][0] = ld_sc1(rs, v0 + (s & 3) * 1024, P2_SOFF(s)); ra[s][1] = ld_sc1(rs, v1 + (s & 3) * 1024, P2_SOFF(s));
  }
  TS(13);
  SCHED();
  spin_ge(M_FULL, 11u);
  vb = P2_BASE(0);
#pragma unroll
  for (int n = 0; n < 5; ++n) wf[0][n] = P2_READ(0, n);
  SCHED();
#pragma unroll
  for (int s = 0; s < NS; ++s) {
    const int r0 = ((s & 3) == 3) ? 2 : 1;
#pragma unroll
    for (int j = 0; j < 10; ++j) {
      acc[j & 1][j >> 1] = __builtin_amdgcn_mfma_f32_16x16x32_bf16(wf[s & 1][j >> 1], __builtin_bit_cast(bf16x8, ra[s % DP][j & 1]), acc[j & 1][j >> 1], 0, 0, 0);
      SCHED();
      if (j == 0 && s + 1 < NS && (ROLE || (s & 3) == 3)) vb = P2_BASE(s + 1);
      if ((s & 3) == 3 && j == 1) {
        WAIT_L(0);
        lds_st1(vprog, 21 + (s >> 2));
        TS(40 + (s >> 2));
        if (s + 1 < NS) { if (__builtin_amdgcn_readfirstlane(fl) < 12u + (s >> 2)) spin_ge(M_FULL + (((s >> 2) + 1) & 1) * 4, 12u + (s >> 2)); }
      }
      if (s + 1 < NS && j >= r0 && j < r0 + 5) wf[(s + 1) & 1][j - r0] = P2_READ(s + 1, j - r0);
      if ((s & 3) == 2 && j == 6 && s + 2 < NS) fl = lds_ld(M_FULL + (((s >> 2) + 1) & 1) * 4);
      if (j == 7 || j == 8) {
        const int sl = s - 1 + DP;
        if (sl < NS && j == 7) P2_RDY(sl);
        if (sl < NS) ra[sl % DP][j - 7] = ld_sc1(rs, (j == 7 ? v0 : v1) + (sl & 3) * 1024, P2_SOFF(sl));
        else if (ROLE && s >= 28 && s < 33) xq[s - 28][j - 7] = *(const GAS u32x2*)(xrow + (j - 7) * 1280 + (s - 28) * 32);
        else if (ROLE && s >= 35) gq[s - 35][j - 7] = (u32x4){0u, ~tag, 0u, ~tag};
      }
      SCHED();
    }
  }
#undef P2_SOFF
#undef P2_RDY
#undef P2_BASE
#undef P2_READ
  TS(7);
  if (ROLE == 0) {
    const float kk = silu_k();
#pragma unroll
    for (int n = 0; n < 5; ++n)
#pragma unroll
      for (int m = 0; m < 2; ++m) {
        const u32x2 s = silu_pack4(acc[m][n], kk);
        st_sc1((u32x4){s[0], tag, s[1], tag}, rs, gv + n * 2048 + m * 1024, 0);
      }
  } else {
#pragma clang loop unroll(disable)
    for (unsigned sp = 0; sp < SPIN_CAP; ++sp) {
      bool ok = true;
#pragma unroll
      for (int n = 0; n < 5; ++n)
#pragma unroll
        for (int m = 0; m < 2; ++m) ok &= (gq[n][m][1] == tag) & (gq[n][m][3] == tag);
      if (__all(ok)) break;
      TS(18);
#pragma clang loop unroll(disable)
      for (; sp < SPIN_CAP; ++sp) {
        MEMBAR();
        const u32x4 t = ld_sc1(rs, gv - lane * 16 + POLL_OFF, 0);
        if (__builtin_amdgcn_readfirstlane(t[1]) == tag) break;
        __builtin_amdgcn_s_sleep(2);
      }
      TS(19);
      MEMBAR();
#pragma unroll
      for (int n = 0; n < 5; ++n) { gq[n][0] = ld_sc1(rs, gv + n * 2048, 0); gq[n][1] = ld_sc1(rs, gv + n * 2048 + 1024, 0); }
    }
    TS(17);
    GAS char* orow = (GAS char*)P.out + (size_t)(128 * G.c + tl) * 2560 + (80 * ct + 4 * g) * 4;
#pragma unroll
    for (int n = 0; n < 5; ++n)
#pragma unroll
      for (int m = 0; m < 2; ++m) {
        const f32x4 xf = {lo_f(xq[n][m][0]), hi_f(xq[n][m][0]), lo_f(xq[n][m][1]), hi_f(xq[n][m][1])};
        const f32x4 gf = {lo_f(gq[n][m][0]), hi_f(gq[n][m][0]), lo_f(gq[n][m][2]), hi_f(gq[n][m][2])};
        *(GAS f32x4*)(orow + m * 2560 + n * 64) = xf + (acc[m][n] * 2.f) * gf;
      }
  }
  TS(8);
}

template <int ROLE>
__device__ __forceinline__ void loader1(const Params& P, Geo& G, rsrc_t rs, int H, int lane) {
  constexpr int NPB = ROLE ? 5 : 0, NCG = ROLE ? 0 : 3;
  constexpr int NG = 4 + NPB + NCG, NS = NG + NCG;
  const int r8 = lane >> 3, c8 = lane & 7, h = H & 1, hh = H >> 1;
  const rsrc_t rX = mk_rsrc(P.x, 2048 * 1280);
  const rsrc_t rW = ROLE ? mk_rsrc(hh ? P.Bim : P.Bre, 640 * 1280) : mk_rsrc(P.w1, 640 * 3072);
  const int vA = 4 * r8 * 1280 + ((c8 ^ r8) * 16);
  const int vB = (4 * h + 8 * (r8 >> 2) + (r8 & 3)) * 1280 + ((c8 ^ r8) * 16), vM = (8 * h + r8) * 1280 + ((c8 ^ r8) * 16);
  const int vF = (8 * h + r8) * 3072 + ((((c8 >> 1) ^ (((r8 >> 1) & 1) | (h << 1))) << 1) | (c8 & 1)) * 16;
  const int sA = (G.c * 128 + 32 * h + hh) * 1280, sB = 80 * G.a * 1280, sF = 384 * G.a + 16 * hh * 3072;
  const unsigned lb = H * 1024, vfull = M_FULL + H * 4;
  auto issueF = [&](int s, int x) {
    int f = 2 * s + x; f = f >= 14 ? f - 14 : f >= 7 ? f - 7 : f;
#pragma unroll
    for (int k = 0; k < NCG; ++k) dma(rW, vF, sF + (64 * s + 32 * x) * 3072 + k * 128, lb + 65536 + f * 12288 + k * 4096);
  };
  auto issueG = [&](int s) {
    const int sl = s & 3;
#pragma unroll
    for (int k = 0; k < 4; ++k) dma(rX, vA, sA + (64 * (k >> 1) + 2 * (k & 1)) * 1280 + 128 * s, lb + sl * 16384 + k * 4096);
#pragma unroll
    for (int k = 0; k < NPB; ++k) dma(rW, k == 2 ? vM : vB, sB + 16 * k * 1280 + 128 * s, lb + 65536 + sl * 20480 + k * 4096);
    issueF(s, 0);
  };
  unsigned ep = 0u;
  if (!ROLE) ep = ld_epoch(rs);
  issueG(0);
  BAR();
  issueF(0, 1); issueG(1); issueF(1, 1); issueG(2); issueF(2, 1);
  TS(1);
  WAIT_V(2 * NS);
  lds_st1(vfull, 1u);
  TS(10);
  issueG(3);
  WAIT_V(NS + NG);
  lds_st1(vfull, 2u);
  TS(11);
  if (!ROLE && H == 3) { lds_st1(M_TAG, ep + 1u); lds_st1(M_SVCB, 2u); }
  if (!ROLE) { spin_prog(1); issueF(3, 1); }
#pragma unroll 1
  for (int s = 4; s < 10; ++s) {
    spin_prog(2 * s - 6);
    TS(20 + s);
    issueG(s);
    TS(30 + s);
    WAIT_V(NS + NG);
    lds_st1(vfull, s - 1);
    TS(40 + s);
    if (!ROLE) { spin_prog(2 * s - 5); issueF(s, 1); }
  }
}
template <int ROLE>
__device__ __forceinline__ void loader2(const Params& P, Geo& G, int H, int lane, int ct, int q0) {
  constexpr int NQ = ROLE ? 10 : 12;
  const int r8 = lane >> 3, c8 = lane & 7;
  const rsrc_t r0 = ROLE ? mk_rsrc(P.Cre, 640 * 1280) : mk_rsrc(P.w2, 1536 * 1280), r1 = ROLE ? mk_rsrc(P.Cim, 640 * 1280) : r0;
  int vo[5];
  if (ROLE) vo[0] = r8 * 1280 + ((c8 ^ r8) * 16);
  else {
#pragma unroll
    for (int v = 0; v < 5; ++v) { const int t = 64 * v + lane; vo[v] = (t / 10) * 1280 + (t % 10) * 16; }
  }
  auto issue = [&](int q) {
    const int qq = q + q0 >= NQ ? q + q0 - NQ : q + q0;
    const int sl = (q >= 7 ? q - 7 : q) * SLOT2;
#pragma unroll
    for (int j = 0; j < 20; ++j) {
      if (ROLE) dma(j >= 10 ? r1 : r0, vo[0], (80 * ct + 8 * (j % 10)) * 1280 + 128 * qq, sl + j * 1024);
      else dma(r0, vo[j % 5], (128 * qq + 32 * (j / 5)) * 1280 + 160 * ct, sl + j * 1024);
    }
  };
  spin_prog(20);
  TS(12);
  int qi = H, qp = H;
#pragma unroll 1
  while (qp < NQ) {
    if (qi < NQ) {
      const int depth = (__builtin_amdgcn_readfirstlane(lds_ld(M_GO)) != 0u) ? 6 : L2_DEPTH;
      if (qi - qp < depth) {
        if (qi == qp) spin_prog(14 + qi);
        if (qi == qp || read_prog() >= (unsigned)(14 + qi)) { issue(qi); qi += 2; continue; }
      }
    }
    if (qi - qp == 2) WAIT_V(0); else if (qi - qp == 4) WAIT_V(20); else WAIT_V(40);
    lds_st1(M_FULL + H * 4, 11 + qp);
    TS(50 + qp);
    qp += 2;
  }
}

template <int ROLE, int PART>
__device__ __forceinline__ void gather(Geo& G, rsrc_t rs, int lane) {
  constexpr int NS = ROLE ? 9 : 10, NC = ROLE ? 2 : 10, NL = NC + (PART ? 0 : 2);
  const int vs = W_STAT + (128 * G.c + lane) * 16;
  const int vc = ROLE ? W_CSB + (80 * G.a + lane) * 16 : W_CSF + (192 * G.a + (PART ? 128 + 2 * (lane & 31) : 2 * lane)) * 8;
  const int vc1 = W_CSB + (80 * G.a + 64 + (lane & 15)) * 16;
  u32x4 s0, s1, cf[NC];
#define G_LOAD() do { \
    if (!PART) { s0 = ld_sc1(rs, vs, 0); s1 = ld_sc1(rs, vs + 1024, 0); } \
    if (ROLE) { cf[0] = ld_sc1(rs, vc, 0); cf[1] = ld_sc1(rs, vc1, 0); } \
    else { _Pragma("unroll") for (int k = 0; k < NC; ++k) cf[k] = ld_sc1(rs, vc + k * 12288, 0); } \
  } while (0)
  G_LOAD();
  SCHED();
  WAIT_V(NS + NL);
  lds_st1(M_FULL + 8 + 4 * PART, 9u);
  WAIT_V(NL);
  lds_st1(M_FULL + 8 + 4 * PART, 10u);
  spin_ge(M_SVCB, 2u);
  const unsigned tag = lds_ld(M_TAG);
#pragma clang loop unroll(disable)
  for (unsigned sp = 0; sp < SPIN_CAP; ++sp) {
    bool ok = true;
    if (!PART) ok = (s0[1] == tag) & (s0[3] == tag) & (s1[1] == tag) & (s1[3] == tag);
#pragma unroll
    for (int k = 0; k < NC; ++k) ok &= (cf[k][1] == tag) & (cf[k][3] == tag);
    if (__all(ok)) break;
    __builtin_amdgcn_s_sleep(4);
    MEMBAR();
    G_LOAD();
  }
#undef G_LOAD
  if (!PART) {
    *(LAS u32x2*)(size_t)(M_STAT + lane * 8) = (u32x2){s0[0], s0[2]};
    *(LAS u32x2*)(size_t)(M_STAT + 512 + lane * 8) = (u32x2){s1[0], s1[2]};
  }
  if (ROLE) {
    *(LAS unsigned*)(size_t)(M_CS + chpos(lane) * 4) = cf[0][0];
    *(LAS unsigned*)(size_t)(M_CS + 320 + chpos(lane) * 4) = cf[0][2];
    *(LAS unsigned*)(size_t)(M_CS + chpos(64 + (lane & 15)) * 4) = cf[1][0];
    *(LAS unsigned*)(size_t)(M_CS + 320 + chpos(64 + (lane & 15)) * 4) = cf[1][2];
  } else {
    float c0 = 0.f, c1 = 0.f;
#pragma unroll
    for (int k = 0; k < NC; ++k) { c0 += __uint_as_float(cf[k][0]); c1 += __uint_as_float(cf[k][2]); }
    *(LAS f32x2*)(size_t)(M_CS + (PART ? 128 + 2 * (lane & 31) : 2 * lane) * 4) = (f32x2){c0, c1};
  }
  WAIT_L(0);
  lds_inc(M_SVCA);
  TS(2);
}
__device__ __forceinline__ void service_go(Geo& G, rsrc_t rs, int lane, int q0) {
  const unsigned tag = lds_ld(M_TAG);
  const int vfl = W_FLAG + (G.c * 2 + G.role) * 256 + (lane & 31) * 8;
  const unsigned sp_ = G.role ? 5u : 6u, skip = 4u * q0 - sp_ * G.a;
  spin_ge(G.role ? M_CARRY : M_CNTE, G.role ? 2u : 4u);
  unsigned last = 0u;
#pragma clang loop unroll(disable)
  for (unsigned sp = 0; sp < SPIN_CAP; ++sp) {
    const u32x2 f = __builtin_amdgcn_raw_buffer_load_b64(rs, vfl, 0, 16);
    const unsigned m = (unsigned)__ballot((f[0] == tag) & (f[1] == ~tag));
    const unsigned z = ~(__builtin_rotateright32(m, 4u * G.a) | 0xFu);
    const unsigned np = z ? (unsigned)__builtin_ctz(z) >> 2 : 8u;
    const unsigned R = np >= 8u ? 8u * sp_ : np * sp_ - skip;
    MEMBAR();
    if (R > last) { lds_st1(M_GO, R); last = R; }
    if (np >= 8u) break;
    __builtin_amdgcn_s_sleep(2);
  }
  TS(3);
}
__device__ __forceinline__ void lookback(Geo& G, rsrc_t rs, int lane, int part) {
  const int cl = 40 * part + (lane < 40 ? lane : 0);
  const unsigned p4 = chpos(cl) * 4;
  spin_ge(M_SVCB, 2u);
  const unsigned tag = lds_ld(M_TAG);
  const CX<float> t32 = lds_cx<float>(M_TAB + 4 * 640 + p4);
  const f32x2 L32 = {t32.re, t32.im};
  const f32x2 L64 = cmul(L32, L32), L128 = cmul(L64, L64);
  const int ve = lane < 40 ? W_EGR + (80 * G.a + cl) * 16 : 0x7F000000;
  spin_ge(M_CNTE, 4u);
  TS(4);
  const CX<float> a0 = lds_cx<float>(M_ENDW + p4), a1 = lds_cx<float>(M_ENDW + 640 + p4);
  const f32x2 e0 = {a0.re, a0.im}, e1 = {a1.re, a1.im};
  if (G.c < 15) {
    const f32x2 ec = cfma(L64, e0, e1);
    st_sc1((u32x4){__float_as_uint(ec.x), tag, __float_as_uint(ec.y), tag}, rs, ve + G.c * 10240, 0);
  }
  f32x2 S = {0.f, 0.f};
  if (G.c > 0) {
    u32x4 ev[15];
    unsigned pend = (1u << G.c) - 1u;
#pragma clang loop unroll(disable)
    for (unsigned sp = 0; sp < SPIN_CAP; ++sp) {
#pragma unroll
      for (int j = 0; j < 15; ++j)
        if (pend & (1u << j)) ev[j] = ld_sc1(rs, ve + j * 10240, 0);
#pragma unroll
      for (int j = 0; j < 15; ++j)
        if (pend & (1u << j)) { if (__all(((ev[j][1] == tag) & (ev[j][3] == tag)) | (lane >= 40))) pend &= ~(1u << j); }
      if (pend == 0u) break;
      __builtin_amdgcn_s_sleep(1);
      MEMBAR();
    }
#pragma unroll
    for (int j = 0; j < 15; ++j)
      if (j < G.c) S = cfma(L128, S, (f32x2){__uint_as_float(ev[j][0]), __uint_as_float(ev[j][2])});
  }
  if (lane < 40) {
    const f32x2 S1 = cfma(L64, S, e0);
    *(LAS float*)(size_t)(M_SIN + p4) = S.x; *(LAS float*)(size_t)(M_SIN + 320 + p4) = S.y;
    *(LAS float*)(size_t)(M_SIN + 640 + p4) = S1.x; *(LAS float*)(size_t)(M_SIN + 960 + p4) = S1.y;
  }
  WAIT_L(0);
  lds_inc(M_CARRY);
  TS(5);
}
template <int ROLE>
__device__ __forceinline__ void helper(const Params& P, Geo& G, rsrc_t rs, int H, int lane, int q0) {
  constexpr int NS = ROLE ? 9 : 10;
  loader1<ROLE>(P, G, rs, H, lane);
  if (H < 2) {
    const int r = G.u & 31;
    if (H == 0) {
      const rsrc_t rw = mk_rsrc(P.w2, 1536 * 1280);
      const int q = (lane * 52) >> 8;
      const int vo = lane < 60 ? q * 1280 + (lane - 5 * q) * 128 : 0x7F000000;
#pragma unroll
      for (int k = 0; k < 4; ++k) touch(rw, vo, (48 * r + 12 * k) * 1280 + 640 * G.par);
    } else {
      const rsrc_t r0 = mk_rsrc(P.Cre, 640 * 1280), r1 = mk_rsrc(P.Cim, 640 * 1280);
      const int vo = lane < 50 ? lane * 128 : 0x7F000000;
#pragma unroll
      for (int k = 0; k < 4; ++k) touch((k >> 1) ? r1 : r0, vo, (320 * G.par + 10 * r) * 1280 + (k & 1) * 6400);
    }
    WAIT_V(NS + 4);
    lds_st1(M_FULL + H * 4, 9u);
    WAIT_V(4);
    lds_st1(M_FULL + H * 4, 10u);
    loader2<ROLE>(P, G, H, lane, G.a, q0);
  } else {
    if (H == 2) gather<ROLE, 0>(G, rs, lane);
    else if (!ROLE) gather<0, 1>(G, rs, lane);
    else {
      WAIT_V(NS);
      lds_st1(M_FULL + 12, 9u);
      WAIT_V(0);
      lds_st1(M_FULL + 12, 10u);
    }
    if (ROLE) lookback(G, rs, lane, H & 1);
    if (H == 2) service_go(G, rs, lane, q0);
  }
}

extern "C" __global__ __launch_bounds__(512, 2) void s5_fused_kernel(const Params P) {
  __shared__ __attribute__((aligned(1024))) char lds_[163840];
  asm volatile("" ::"v"((LAS char*)lds_) : "memory");
  const int tid = threadIdx.x, wid = __builtin_amdgcn_readfirstlane(tid >> 6), lane = tid & 63;
  const int b = blockIdx.x, xcd = b & 7, r = b >> 3, jj = r >> 2;
  Geo G;
  G.c = 4 * (xcd >> 1) + (r & 3); G.role = jj >> 2; G.par = xcd & 1;
  G.a = 4 * G.par + (jj & 3); G.pid = 8 * G.par + jj; G.u = (xcd >> 1) * 32 + r;
  const int q0 = ((G.role ? 5 : 6) * G.a + 3) >> 2;
  const rsrc_t rs = mk_rsrc(P.ws, W_END);
  TS(0); TC(62);
  if (wid < 4) {
    if (wid == 2) {
      uint64_t pc; unsigned off, len;
      asm volatile("s_getpc_b64 %0\n.Lpf_here%=:\n\ts_mov_b32 %1, .Lpf_here%=-s5_fused_kernel\n\ts_mov_b32 %2, s5_code_end-s5_fused_kernel"
                   : "=s"(pc), "=s"(off), "=s"(len));
      len = (len < 0x40000u && off < len) ? len : 0u;
      const int per = (len >> 12) + 1;
      touch(mk_rsrc((const void*)(pc - off), len), lane < per ? (r * per + lane) * 128 : 0x7F000000, 0);
    }
    if (G.role) {
      if (wid < 2) svc_rows(P, G, rs, lane, wid); else svc_tables(P, G, rs, lane, wid - 2);
      p1_mfma<1>(P, G, rs, wid, lane); p2_mfma<1>(P, G, rs, wid, lane, G.a, q0);
    } else {
      if (wid == 0) { if (lane < 16) *(LAS unsigned*)(size_t)(MISC + lane * 4) = 0u; WAIT_L(0); }
      BAR();
      p1_mfma<0>(P, G, rs, wid, lane); p2_mfma<0>(P, G, rs, wid, lane, G.a, q0);
    }
  } else {
    const int H = wid - 4;
    if (G.role) helper<1>(P, G, rs, H, lane, q0); else helper<0>(P, G, rs, H, lane, q0);
    if (b == 0 && H == 0) {
      const unsigned tag = lds_ld(M_TAG);
#pragma clang loop unroll(disable)
      for (unsigned sp = 0; sp < SPIN_CAP; ++sp) {
        bool ok = true;
#pragma unroll
        for (int k = 0; k < 4; ++k) {
          const u32x2 f = __builtin_amdgcn_raw_buffer_load_b64(rs, W_FLAG + (lane * 4 + k) * 32, 0, 16);
          ok &= (f[0] == tag) & (f[1] == ~tag);
        }
        if (__all(ok)) break;
        __builtin_amdgcn_s_sleep(2);
        MEMBAR();
      }
      if (lane == 0) __builtin_amdgcn_raw_buffer_store_b32(tag, rs, W_EPOCH, 0, 16);
    }
  }
  TS(9); TC(63);
}
extern "C" __device__ __attribute__((used, noinline)) void s5_code_end() {}

extern "C" void kernel_launch(void* const* d_in, const int* in_sizes, int n_in, void* d_out, int out_size, void* d_ws,
                              size_t ws_size, hipStream_t stream) {
  Params P;
  P.x = (const char*)d_in[0]; P.lre = (const char*)d_in[1]; P.lim = (const char*)d_in[2];
  P.Bre = (const char*)d_in[3]; P.Bim = (const char*)d_in[4]; P.Cre = (const char*)d_in[5]; P.Cim = (const char*)d_in[6];
  P.logdt = (const char*)d_in[7]; P.dskip = (const char*)d_in[8]; P.w1 = (const char*)d_in[10]; P.w2 = (const char*)d_in[11];
  P.ws = (char*)d_ws; P.out = (float*)d_out;
  s5_fused_kernel<<<256, 512, 0, stream>>>(P);
}
```
